```python
import torch

def _softmax(x, axis=-1):
    return torch.softmax(x, dim=axis)
def _gelu(x):
    return torch.nn.functional.gelu(x, approximate="tanh")
def _silu(x):
    return torch.nn.functional.silu(x)
def _sigmoid(x):
    return torch.sigmoid(x)
def _layernorm(x, axis=-1, eps=1e-5):
    m = x.mean(dim=axis, keepdim=True)
    v = x.var(dim=axis, keepdim=True, unbiased=False)
    return (x - m) / torch.sqrt(v + eps)
def _scatter_add(tgt, idx, src, axis=0):
    return tgt.clone().index_add_(axis, idx, src)
def _segment_sum(src, seg, n):
    out = torch.zeros((n,) + src.shape[1:], dtype=src.dtype)
    return out.index_add_(0, seg, src)
def _pad(x, pw):
    flat = tuple(v for pair in reversed(pw) for v in pair)
    return torch.nn.functional.pad(x, flat)

kernel_name = 'standalone__gqa_block_cached__near_ridge'

def setup_inputs():

    _w = (lambda a: (lambda t: t.to(torch.bfloat16) if t.is_floating_point() else t)(torch.as_tensor(a)))
    B, Sq, D = 8, 240, 512
    Hq, Hkv, Sk, Dh = 32, 32, 512, 128
    assert Hq % Hkv == 0, (Hq, Hkv)
    return {
        "x": _w((lambda *s: torch.randn(*s, dtype=torch.bfloat16) * 0.8)(B, Sq, D)),
        "k_cache": _w((lambda *s: torch.randn(*s, dtype=torch.bfloat16) * 0.8)(B, Hkv, Sk, Dh)),
        "v_cache": _w((lambda *s: torch.randn(*s, dtype=torch.bfloat16) * 0.05)(B, Hkv, Sk, Dh)),
        "wq": _w((lambda *s: torch.randn(*s, dtype=torch.bfloat16) * 0.8)(D, Hq * Dh) * (D ** -0.5)),
        "wo": _w((lambda *s: torch.randn(*s, dtype=torch.bfloat16) * 0.05)(Hq * Dh, D) * ((Hq * Dh) ** -0.5)),
    }

def reference(x, k_cache, v_cache, wq, wo):
    B, Sq, D = x.shape
    Hq, Dh = 32, 128
    Hkv = k_cache.shape[1]
    rep = Hq // Hkv
    q = (lambda x, *d: x.permute(*d))((x @ wq).reshape(B, Sq, Hq, Dh), 0, 2, 1, 3)
    k = (lambda x, n, axis: torch.repeat_interleave(x, n, dim=axis))(k_cache, rep, axis=1)
    v = (lambda x, n, axis: torch.repeat_interleave(x, n, dim=axis))(v_cache, rep, axis=1)
    scores = torch.einsum("bhqd,bhkd->bhqk", q, k) * (Dh ** -0.5)
    p = _softmax(scores, axis=-1)
    o = torch.einsum("bhqk,bhkd->bhqd", p, v)
    return (lambda x, *d: x.permute(*d))(o, 0, 2, 1, 3).reshape(B, Sq, Hq * Dh) @ wo
```

```cpp
#include <hip/hip_runtime.h>
#include <hip/hip_bf16.h>

__global__ __launch_bounds__(256, 1) void gqa_fused_kernel(const void* x, const void* k, const void* v, const void* wq, const void* wo, void* out, void* ws) {
  __shared__ __attribute__((aligned(16))) char lds[163840];
  unsigned tid = threadIdx.x;
  asm volatile(
"s_mov_b32 s61, %1\n"
"v_mov_b32 v251, %2\n"
"s_load_dwordx8 s[88:95], %0, 0x0\n"
"s_load_dwordx4 s[96:99], %0, 0x20\n"
"s_load_dwordx2 s[100:101], %0, 0x30\n"
"v_lshrrev_b32 v248, 6, v251\n"
"v_and_b32 v251, 63, v251\n"
"v_readfirstlane_b32 s44, v248\n"
"s_and_b32 s54, s61, 7\n"
"s_lshr_b32 s55, s61, 3\n"
"s_lshl_b32 s45, s44, 12\n"
"s_and_b32 s52, s54, 4\n"
"s_and_b32 s56, s55, 3\n"
"s_add_u32 s52, s52, s56\n"
"s_and_b32 s53, s54, 3\n"
"s_lshl_b32 s53, s53, 3\n"
"s_lshr_b32 s56, s55, 2\n"
"s_add_u32 s53, s53, s56\n"
"s_cmp_eq_u32 s44, 0\n"
"s_cselect_b64 s[46:47], -1, 0\n"
"s_mov_b32 s58, 0\n"
"s_mov_b32 s59, 262144\n"
"v_lshrrev_b32 v0, 3, v251\n"
"v_and_b32 v2, 7, v251\n"
"v_lshrrev_b32 v3, 4, v251\n"
"v_xor_b32 v6, v2, v3\n"
"v_lshlrev_b32 v6, 4, v6\n"
"v_and_b32 v3, 1, v3\n"
"v_lshlrev_b32 v3, 2, v3\n"
"v_xor_b32 v5, v2, v3\n"
"v_lshlrev_b32 v5, 4, v5\n"
"s_lshl_b32 s56, s44, 17\n"
"v_lshl_add_u32 v128, v0, 13, v5\n"
"v_add_u32 v128, s56, v128\n"
"s_lshl_b32 s56, s44, 16\n"
"v_lshl_add_u32 v129, v0, 10, v6\n"
"v_add_u32 v129, s56, v129\n"
"v_xor_b32 v130, 64, v129\n"
"v_lshl_add_u32 v224, v0, 8, v6\n"
"v_add_u32 v224, s45, v224\n"
"v_xor_b32 v225, 64, v224\n"
"v_lshl_add_u32 v226, v0, 8, v5\n"
"v_add_u32 v226, s45, v226\n"
"v_lshl_add_u32 v227, v0, 10, v5\n"
"v_lshlrev_b32 v228, 4, v251\n"
"v_add_u32 v252, 7168, v227\n"
"s_and_b32 s56, s55, 3\n"
"s_lshl_b32 s56, s56, 1\n"
"s_lshr_b32 s57, s55, 2\n"
"s_add_u32 s56, s56, s57\n"
"s_and_b32 s60, s56, 7\n"
"s_mul_i32 s56, s60, 2\n"
"s_and_b32 s56, s56, 7\n"
"s_lshl_b32 s48, s56, 7\n"
"s_lshl_b32 s49, s56, 19\n"
"s_getpc_b64 s[16:17]\n"
"s_mov_b32 s18, 39936\n"
"s_mov_b32 s19, 0x20000\n"
"s_lshl_b32 s57, s55, 12\n"
"s_lshl_b32 s56, s44, 10\n"
"s_add_u32 s56, s56, s57\n"
"s_add_u32 m0, s45, 147456\n"
"s_nop 0\n"
"buffer_load_dwordx4 v228, s[16:19], s56 offen lds\n"
"s_waitcnt lgkmcnt(0)\n"
"s_mul_i32 s56, s52, 245760\n"
"s_add_u32 s8, s88, s56\n"
"s_addc_u32 s9, s89, 0\n"
"s_mov_b32 s10, 245760\n"
"s_mov_b32 s11, 0x20000\n"
"s_lshl_b32 s56, s53, 8\n"
"s_add_u32 s12, s94, s56\n"
"s_addc_u32 s13, s95, 0\n"
"s_mov_b32 s14, 4186368\n"
"s_mov_b32 s15, 0x20000\n"
"s_add_u32 m0, s45, 16384\n"
"s_add_u32 s56, s49, 0\n"
"buffer_load_dwordx4 v128, s[12:15], s56 offen lds\n"
"s_add_u32 m0, s45, 17408\n"
"s_add_u32 s56, s49, 128\n"
"buffer_load_dwordx4 v128, s[12:15], s56 offen lds\n"
"s_add_u32 m0, s45, 18432\n"
"s_add_u32 s56, s49, 65536\n"
"buffer_load_dwordx4 v128, s[12:15], s56 offen lds\n"
"s_add_u32 m0, s45, 19456\n"
"s_add_u32 s56, s49, 65664\n"
"buffer_load_dwordx4 v128, s[12:15], s56 offen lds\n"
"s_add_u32 s49, s49, 524288\n"
"s_and_b32 s49, s49, 4194303\n"
"s_add_u32 m0, s45, 81920\n"
"s_add_u32 s56, s48, 0\n"
"buffer_load_dwordx4 v129, s[8:11], s56 offen lds\n"
"s_add_u32 m0, s45, 82944\n"
"s_add_u32 s56, s48, 8192\n"
"buffer_load_dwordx4 v130, s[8:11], s56 offen lds\n"
"s_add_u32 m0, s45, 83968\n"
"s_add_u32 s56, s48, 16384\n"
"buffer_load_dwordx4 v129, s[8:11], s56 offen lds\n"
"s_add_u32 m0, s45, 84992\n"
"s_add_u32 s56, s48, 24576\n"
"buffer_load_dwordx4 v130, s[8:11], s56 offen lds\n"
"s_add_u32 m0, s45, 32768\n"
"s_add_u32 s56, s48, 32768\n"
"buffer_load_dwordx4 v129, s[8:11], s56 offen lds\n"
"s_add_u32 m0, s45, 33792\n"
"s_add_u32 s56, s48, 40960\n"
"buffer_load_dwordx4 v130, s[8:11], s56 offen lds\n"
"s_add_u32 m0, s45, 34816\n"
"s_add_u32 s56, s48, 49152\n"
"buffer_load_dwordx4 v129, s[8:11], s56 offen lds\n"
"s_add_u32 m0, s45, 35840\n"
"s_add_u32 s56, s48, 57344\n"
"buffer_load_dwordx4 v130, s[8:11], s56 offen lds\n"
"s_add_u32 s48, s48, 128\n"
"s_and_b32 s48, s48, 1023\n"
"s_lshl_b32 s56, s52, 5\n"
"s_add_u32 s56, s56, s53\n"
"s_lshl_b32 s57, s56, 17\n"
"s_add_u32 s16, s90, s57\n"
"s_addc_u32 s17, s91, 0\n"
"s_mov_b32 s18, 131072\n"
"s_mov_b32 s19, 0x20000\n"
"s_add_u32 s20, s92, s57\n"
"s_addc_u32 s21, s93, 0\n"
"s_mov_b32 s22, 131072\n"
"s_mov_b32 s23, 0x20000\n"
"s_xor_b32 s86, s61, s55\n"
"s_and_b32 s86, s86, 7\n"
"s_lshl_b32 s86, s86, 14\n"
"s_and_b32 s85, s54, 6\n"
"s_bfe_u32 s56, s55, 0x10002\n"
"s_add_u32 s85, s85, s56\n"
"s_and_b32 s87, s55, 7\n"
"s_and_b32 s56, s54, 1\n"
"s_lshl_b32 s56, s56, 2\n"
"s_lshr_b32 s57, s55, 3\n"
"s_add_u32 s56, s56, s57\n"
"s_lshl_b32 s56, s56, 7\n"
"s_mov_b32 s84, s56\n"
"s_lshl_b32 s57, s44, 20\n"
"s_add_u32 s56, s56, s57\n"
"s_add_u32 s24, s96, s56\n"
"s_addc_u32 s25, s97, 0\n"
"s_mov_b32 s26, 1047680\n"
"s_mov_b32 s27, 0x20000\n"
"s_lshl_b32 s56, s52, 2\n"
"s_add_u32 s56, s56, s44\n"
"s_lshl_b32 s56, s56, 19\n"
"s_lshl_b32 s57, s53, 14\n"
"s_add_u32 s56, s56, s57\n"
"s_add_u32 s56, s56, 1048576\n"
"s_add_u32 s28, s100, s56\n"
"s_addc_u32 s29, s101, 0\n"
"s_mov_b32 s30, 16384\n"
"s_mov_b32 s31, 0x20000\n"
"s_lshl_b32 s56, s85, 2\n"
"s_and_b32 s57, s55, 3\n"
"s_add_u32 s56, s56, s57\n"
"s_lshl_b32 s56, s56, 19\n"
"s_lshl_b32 s57, s44, 17\n"
"s_add_u32 s56, s56, s57\n"
"s_add_u32 s56, s56, 1048576\n"
"s_add_u32 s32, s100, s56\n"
"s_addc_u32 s33, s101, 0\n"
"s_mov_b32 s34, 131072\n"
"s_mov_b32 s35, 0x20000\n"
"s_mul_i32 s56, s85, 245760\n"
"s_add_u32 s36, s98, s56\n"
"s_addc_u32 s37, s99, 0\n"
"s_mov_b32 s38, 245760\n"
"s_mov_b32 s39, 0x20000\n"
"s_mov_b32 s56, 0\n"
"s_add_u32 s40, s100, s56\n"
"s_addc_u32 s41, s101, 0\n"
"s_mov_b32 s42, 1048576\n"
"s_mov_b32 s43, 0x20000\n"
"s_and_b32 s56, s85, 4\n"
"s_and_b32 s57, s85, 3\n"
"s_lshl_b32 s57, s57, 3\n"
"s_add_u32 s56, s56, s57\n"
"v_bfe_u32 v0, v251, 2, 3\n"
"v_and_b32 v1, 3, v251\n"
"v_lshl_add_u32 v1, v0, 5, v1\n"
"v_add_u32 v1, s56, v1\n"
"v_and_b32 v2, 1, v251\n"
"v_bfe_u32 v3, v251, 1, 2\n"
"v_lshl_add_u32 v2, v3, 3, v2\n"
"v_bfe_u32 v3, v251, 3, 2\n"
"v_lshl_add_u32 v2, v3, 6, v2\n"
"s_and_b32 s56, s52, 1\n"
"s_lshl_b32 s56, s56, 5\n"
"s_and_b32 s57, s52, 6\n"
"s_add_u32 s56, s56, s57\n"
"v_add_u32 v2, s56, v2\n"
"v_cmp_gt_u32 vcc, 32, v251\n"
"s_nop 1\n"
"v_cndmask_b32 v1, v2, v1, vcc\n"
"s_lshl_b32 s56, s61, 2\n"
"v_lshlrev_b32 v245, 10, v1\n"
"v_add_u32 v245, s56, v245\n"
"s_lshl_b32 s56, s61, 10\n"
"v_lshlrev_b32 v246, 2, v1\n"
"v_add_u32 v246, s56, v246\n"
"v_bfe_u32 v0, v251, 3, 2\n"
"v_and_b32 v1, 7, v251\n"
"v_lshrrev_b32 v2, 5, v251\n"
"v_bfe_u32 v3, v251, 1, 3\n"
"v_xor_b32 v4, v2, v3\n"
"v_lshlrev_b32 v4, 4, v4\n"
"v_lshl_add_u32 v4, v1, 7, v4\n"
"v_lshl_add_u32 v229, v0, 11, v4\n"
"v_xor_b32 v230, 32, v229\n"
"v_xor_b32 v231, 64, v229\n"
"v_xor_b32 v232, 96, v229\n"
"v_lshl_add_u32 v4, v0, 10, v4\n"
"v_add_u32 v131, s45, v4\n"
"v_xor_b32 v132, 32, v131\n"
"v_xor_b32 v133, 64, v131\n"
"v_xor_b32 v134, 96, v131\n"
"v_add_u32 v135, 0x10000, v131\n"
"v_add_u32 v136, 0x10000, v132\n"
"v_add_u32 v137, 0x10000, v133\n"
"v_add_u32 v138, 0x10000, v134\n"
"v_bfe_u32 v3, v251, 2, 2\n"
"v_bfe_u32 v5, v251, 4, 1\n"
"v_lshlrev_b32 v5, 5, v5\n"
"v_lshl_add_u32 v5, v3, 7, v5\n"
"v_bfe_u32 v6, v251, 3, 1\n"
"v_lshl_add_u32 v5, v6, 6, v5\n"
"v_and_b32 v6, 3, v251\n"
"v_lshl_add_u32 v7, v6, 3, v5\n"
"v_lshl_add_u32 v7, v2, 9, v7\n"
"v_add_u32 v233, 0x10000, v7\n"
"v_xor_b32 v234, 64, v233\n"
"v_add_u32 v253, 0x20000, v7\n"
"v_xor_b32 v254, 64, v253\n"
"v_and_b32 v6, 1, v251\n"
"v_lshl_add_u32 v5, v6, 4, v5\n"
"v_bfe_u32 v6, v251, 1, 1\n"
"v_lshl_add_u32 v5, v6, 3, v5\n"
"v_lshl_add_u32 v5, v2, 11, v5\n"
"v_add_u32 v139, 0x10000, v5\n"
"v_xor_b32 v140, 64, v139\n"
"v_add_u32 v141, 0x20000, v5\n"
"v_xor_b32 v142, 64, v141\n"
"v_mov_b32 v143, v5\n"
"v_xor_b32 v144, 64, v5\n"
"v_add_u32 v145, s45, v228\n"
"v_add_u32 v146, 0x10000, v145\n"
"s_add_u32 m0, s45, 49152\n"
"s_add_u32 s56, s48, 0\n"
"buffer_load_dwordx4 v129, s[8:11], s56 offen lds\n"
"s_add_u32 m0, s45, 50176\n"
"s_add_u32 s56, s48, 8192\n"
"buffer_load_dwordx4 v130, s[8:11], s56 offen lds\n"
"s_add_u32 m0, s45, 51200\n"
"s_add_u32 s56, s48, 16384\n"
"buffer_load_dwordx4 v129, s[8:11], s56 offen lds\n"
"s_add_u32 m0, s45, 52224\n"
"s_add_u32 s56, s48, 24576\n"
"buffer_load_dwordx4 v130, s[8:11], s56 offen lds\n"
"s_add_u32 m0, s45, 114688\n"
"s_add_u32 s56, s48, 32768\n"
"buffer_load_dwordx4 v129, s[8:11], s56 offen lds\n"
"s_add_u32 m0, s45, 115712\n"
"s_add_u32 s56, s48, 40960\n"
"buffer_load_dwordx4 v130, s[8:11], s56 offen lds\n"
"s_add_u32 m0, s45, 116736\n"
"s_add_u32 s56, s48, 49152\n"
"buffer_load_dwordx4 v129, s[8:11], s56 offen lds\n"
"s_add_u32 m0, s45, 117760\n"
"s_add_u32 s56, s48, 57344\n"
"buffer_load_dwordx4 v130, s[8:11], s56 offen lds\n"
"s_add_u32 s48, s48, 128\n"
"s_and_b32 s48, s48, 1023\n"
"s_add_u32 m0, s45, 98304\n"
"s_add_u32 s56, s49, 0\n"
"buffer_load_dwordx4 v128, s[12:15], s56 offen lds\n"
"s_add_u32 m0, s45, 99328\n"
"s_add_u32 s56, s49, 128\n"
"buffer_load_dwordx4 v128, s[12:15], s56 offen lds\n"
"s_add_u32 m0, s45, 100352\n"
"s_add_u32 s56, s49, 65536\n"
"buffer_load_dwordx4 v128, s[12:15], s56 offen lds\n"
"s_add_u32 m0, s45, 101376\n"
"s_add_u32 s56, s49, 65664\n"
"buffer_load_dwordx4 v128, s[12:15], s56 offen lds\n"
"s_add_u32 s49, s49, 524288\n"
"s_and_b32 s49, s49, 4194303\n"
"s_waitcnt vmcnt(12)\n"
"s_barrier\n"
"ds_read_b128 a[192:195], v135 offset:16384\n"
"ds_read_b128 a[208:211], v131 offset:32768\n"
"ds_read_b64_tr_b16 a[128:129], v143 offset:16384\n"
"ds_read_b64_tr_b16 a[130:131], v143 offset:16896\n"
"ds_read_b64_tr_b16 a[132:133], v144 offset:16384\n"
"ds_read_b64_tr_b16 a[134:135], v144 offset:16896\n"
"ds_read_b64_tr_b16 a[136:137], v143 offset:17408\n"
"ds_read_b64_tr_b16 a[138:139], v143 offset:17920\n"
"ds_read_b64_tr_b16 a[140:141], v144 offset:17408\n"
"ds_read_b64_tr_b16 a[142:143], v144 offset:17920\n"
"ds_read_b64_tr_b16 a[144:145], v143 offset:20480\n"
"ds_read_b64_tr_b16 a[146:147], v143 offset:20992\n"
"ds_read_b64_tr_b16 a[148:149], v144 offset:20480\n"
"ds_read_b64_tr_b16 a[150:151], v144 offset:20992\n"
"ds_read_b64_tr_b16 a[152:153], v143 offset:21504\n"
"ds_read_b64_tr_b16 a[154:155], v143 offset:22016\n"
"ds_read_b128 a[196:199], v136 offset:16384\n"
"ds_read_b128 a[212:215], v132 offset:32768\n"
"ds_read_b128 a[200:203], v137 offset:16384\n"
"ds_read_b128 a[216:219], v133 offset:32768\n"
"ds_read_b128 a[204:207], v138 offset:16384\n"
"ds_read_b128 a[220:223], v134 offset:32768\n"
"s_waitcnt lgkmcnt(15)\n"
"v_mfma_f32_32x32x16_bf16 v[0:15], a[128:131], a[192:195], 0\n"
"ds_read_b64_tr_b16 a[156:157], v144 offset:21504\n"
"s_add_u32 m0, s45, 0\n"
"s_add_u32 s56, s48, 0\n"
"buffer_load_dwordx4 v129, s[8:11], s56 offen lds\n"
"v_mfma_f32_32x32x16_bf16 v[16:31], a[128:131], a[208:211], 0\n"
"ds_read_b64_tr_b16 a[158:159], v144 offset:22016\n"
"s_add_u32 m0, s45, 1024\n"
"s_add_u32 s56, s48, 8192\n"
"buffer_load_dwordx4 v130, s[8:11], s56 offen lds\n"
"v_mfma_f32_32x32x16_bf16 v[32:47], a[132:135], a[192:195], 0\n"
"ds_read_b64_tr_b16 a[160:161], v143 offset:24576\n"
"s_waitcnt lgkmcnt(3)\n"
"v_accvgpr_read_b32 v248, a223\n"
"v_mfma_f32_32x32x16_bf16 v[48:63], a[132:135], a[208:211], 0\n"
"ds_read_b64_tr_b16 a[162:163], v143 offset:25088\n"
"s_add_u32 m0, s45, 2048\n"
"s_add_u32 s56, s48, 16384\n"
"buffer_load_dwordx4 v129, s[8:11], s56 offen lds\n"
"v_mfma_f32_32x32x16_bf16 v[64:79], a[136:139], a[192:195], 0\n"
"ds_read_b64_tr_b16 a[164:165], v144 offset:24576\n"
"v_mfma_f32_32x32x16_bf16 v[80:95], a[136:139], a[208:211], 0\n"
"ds_read_b64_tr_b16 a[166:167], v144 offset:25088\n"
"s_add_u32 m0, s45, 3072\n"
"s_add_u32 s56, s48, 24576\n"
"buffer_load_dwordx4 v130, s[8:11], s56 offen lds\n"
"v_mfma_f32_32x32x16_bf16 v[96:111], a[140:143], a[192:195], 0\n"
"ds_read_b64_tr_b16 a[168:169], v143 offset:25600\n"
"v_mfma_f32_32x32x16_bf16 v[112:127], a[140:143], a[208:211], 0\n"
"ds_read_b64_tr_b16 a[170:171], v143 offset:26112\n"
"s_add_u32 m0, s45, 65536\n"
"s_add_u32 s56, s48, 32768\n"
"buffer_load_dwordx4 v129, s[8:11], s56 offen lds\n"
"v_mfma_f32_32x32x16_bf16 v[0:15], a[144:147], a[196:199], v[0:15]\n"
"ds_read_b64_tr_b16 a[172:173], v144 offset:25600\n"
"v_mfma_f32_32x32x16_bf16 v[16:31], a[144:147], a[212:215], v[16:31]\n"
"s_add_u32 m0, s45, 66560\n"
"s_add_u32 s56, s48, 40960\n"
"buffer_load_dwordx4 v130, s[8:11], s56 offen lds\n"
"v_mfma_f32_32x32x16_bf16 v[32:47], a[148:151], a[196:199], v[32:47]\n"
"ds_read_b64_tr_b16 a[174:175], v144 offset:26112\n"
"s_add_u32 m0, s45, 67584\n"
"s_add_u32 s56, s48, 49152\n"
"buffer_load_dwordx4 v129, s[8:11], s56 offen lds\n"
"v_mfma_f32_32x32x16_bf16 v[48:63], a[148:151], a[212:215], v[48:63]\n"
"ds_read_b64_tr_b16 a[176:177], v143 offset:28672\n"
"v_mfma_f32_32x32x16_bf16 v[64:79], a[152:155], a[196:199], v[64:79]\n"
"ds_read_b64_tr_b16 a[178:179], v143 offset:29184\n"
"s_waitcnt vmcnt(15)\n"
"ds_read_b128 a[224:227], v131 offset:49152\n"
"ds_read_b128 a[228:231], v132 offset:49152\n"
"s_add_u32 m0, s45, 68608\n"
"s_add_u32 s56, s48, 57344\n"
"buffer_load_dwordx4 v130, s[8:11], s56 offen lds\n"
"s_add_u32 s48, s48, 128\n"
"s_and_b32 s48, s48, 1023\n"
"v_mfma_f32_32x32x16_bf16 v[80:95], a[152:155], a[212:215], v[80:95]\n"
"ds_read_b64_tr_b16 a[180:181], v144 offset:28672\n"
"ds_read_b128 a[232:235], v133 offset:49152\n"
"s_waitcnt lgkmcnt(7)\n"
"v_mfma_f32_32x32x16_bf16 v[96:111], a[156:159], a[196:199], v[96:111]\n"
"ds_read_b64_tr_b16 a[182:183], v144 offset:29184\n"
"ds_read_b128 a[236:239], v134 offset:49152\n"
"s_add_u32 m0, s45, 131072\n"
"s_add_u32 s56, s49, 0\n"
"buffer_load_dwordx4 v128, s[12:15], s56 offen lds\n"
"v_mfma_f32_32x32x16_bf16 v[112:127], a[156:159], a[212:215], v[112:127]\n"
"ds_read_b64_tr_b16 a[184:185], v143 offset:29696\n"
"v_mfma_f32_32x32x16_bf16 v[0:15], a[160:163], a[200:203], v[0:15]\n"
"ds_read_b64_tr_b16 a[186:187], v143 offset:30208\n"
"s_add_u32 m0, s45, 132096\n"
"s_add_u32 s56, s49, 128\n"
"buffer_load_dwordx4 v128, s[12:15], s56 offen lds\n"
"v_mfma_f32_32x32x16_bf16 v[16:31], a[160:163], a[216:219], v[16:31]\n"
"ds_read_b64_tr_b16 a[188:189], v144 offset:29696\n"
"v_mfma_f32_32x32x16_bf16 v[32:47], a[164:167], a[200:203], v[32:47]\n"
"ds_read_b64_tr_b16 a[190:191], v144 offset:30208\n"
"s_waitcnt lgkmcnt(4)\n"
"v_accvgpr_read_b32 v248, a239\n"
"s_waitcnt vmcnt(14)\n"
"ds_read_b128 a[240:243], v135 offset:49152\n"
"ds_read_b128 a[244:247], v136 offset:49152\n"
"s_add_u32 m0, s45, 133120\n"
"s_add_u32 s56, s49, 65536\n"
"buffer_load_dwordx4 v128, s[12:15], s56 offen lds\n"
"v_mfma_f32_32x32x16_bf16 v[48:63], a[164:167], a[216:219], v[48:63]\n"
"ds_read_b128 a[248:251], v137 offset:49152\n"
"s_add_u32 m0, s45, 134144\n"
"s_add_u32 s56, s49, 65664\n"
"buffer_load_dwordx4 v128, s[12:15], s56 offen lds\n"
"s_add_u32 s49, s49, 524288\n"
"s_and_b32 s49, s49, 4194303\n"
"v_mfma_f32_32x32x16_bf16 v[64:79], a[168:171], a[200:203], v[64:79]\n"
"ds_read_b128 a[252:255], v138 offset:49152\n"
"v_mfma_f32_32x32x16_bf16 v[80:95], a[168:171], a[216:219], v[80:95]\n"
"s_add_u32 m0, s45, 81920\n"
"s_add_u32 s56, s48, 0\n"
"buffer_load_dwordx4 v129, s[8:11], s56 offen lds\n"
"v_mfma_f32_32x32x16_bf16 v[96:111], a[172:175], a[200:203], v[96:111]\n"
"v_mfma_f32_32x32x16_bf16 v[112:127], a[172:175], a[216:219], v[112:127]\n"
"s_add_u32 m0, s45, 82944\n"
"s_add_u32 s56, s48, 8192\n"
"buffer_load_dwordx4 v130, s[8:11], s56 offen lds\n"
"v_mfma_f32_32x32x16_bf16 v[0:15], a[176:179], a[204:207], v[0:15]\n"
"s_waitcnt lgkmcnt(0)\n"
"v_accvgpr_read_b32 v248, a255\n"
"s_waitcnt vmcnt(14)\n"
"s_barrier\n"
"ds_read_b64_tr_b16 a[128:129], v139 offset:32768\n"
"ds_read_b64_tr_b16 a[130:131], v139 offset:33280\n"
"v_mfma_f32_32x32x16_bf16 v[16:31], a[176:179], a[220:223], v[16:31]\n"
"ds_read_b64_tr_b16 a[132:133], v140 offset:32768\n"
"ds_read_b64_tr_b16 a[134:135], v140 offset:33280\n"
"s_add_u32 m0, s45, 83968\n"
"s_add_u32 s56, s48, 16384\n"
"buffer_load_dwordx4 v129, s[8:11], s56 offen lds\n"
"v_mfma_f32_32x32x16_bf16 v[32:47], a[180:183], a[204:207], v[32:47]\n"
"ds_read_b64_tr_b16 a[136:137], v139 offset:33792\n"
"ds_read_b64_tr_b16 a[138:139], v139 offset:34304\n"
"v_mfma_f32_32x32x16_bf16 v[48:63], a[180:183], a[220:223], v[48:63]\n"
"ds_read_b64_tr_b16 a[140:141], v140 offset:33792\n"
"s_add_u32 m0, s45, 84992\n"
"s_add_u32 s56, s48, 24576\n"
"buffer_load_dwordx4 v130, s[8:11], s56 offen lds\n"
"v_mfma_f32_32x32x16_bf16 v[64:79], a[184:187], a[204:207], v[64:79]\n"
"ds_read_b64_tr_b16 a[142:143], v140 offset:34304\n"
"ds_read_b64_tr_b16 a[144:145], v139 offset:36864\n"
"s_add_u32 m0, s45, 32768\n"
"s_add_u32 s56, s48, 32768\n"
"buffer_load_dwordx4 v129, s[8:11], s56 offen lds\n"
"v_mfma_f32_32x32x16_bf16 v[80:95], a[184:187], a[220:223], v[80:95]\n"
"ds_read_b64_tr_b16 a[146:147], v139 offset:37376\n"
"ds_read_b64_tr_b16 a[148:149], v140 offset:36864\n"
"v_mfma_f32_32x32x16_bf16 v[96:111], a[188:191], a[204:207], v[96:111]\n"
"ds_read_b64_tr_b16 a[150:151], v140 offset:37376\n"
"ds_read_b64_tr_b16 a[152:153], v139 offset:37888\n"
"s_add_u32 m0, s45, 33792\n"
"s_add_u32 s56, s48, 40960\n"
"buffer_load_dwordx4 v130, s[8:11], s56 offen lds\n"
"v_mfma_f32_32x32x16_bf16 v[112:127], a[188:191], a[220:223], v[112:127]\n"
"ds_read_b64_tr_b16 a[154:155], v139 offset:38400\n"
"s_waitcnt lgkmcnt(9)\n"
"v_mfma_f32_32x32x16_bf16 v[0:15], a[128:131], a[224:227], v[0:15]\n"
"ds_read_b64_tr_b16 a[156:157], v140 offset:37888\n"
"s_add_u32 m0, s45, 34816\n"
"s_add_u32 s56, s48, 49152\n"
"buffer_load_dwordx4 v129, s[8:11], s56 offen lds\n"
"v_mfma_f32_32x32x16_bf16 v[16:31], a[128:131], a[240:243], v[16:31]\n"
"ds_read_b64_tr_b16 a[158:159], v140 offset:38400\n"
"v_mfma_f32_32x32x16_bf16 v[32:47], a[132:135], a[224:227], v[32:47]\n"
"ds_read_b64_tr_b16 a[160:161], v139 offset:40960\n"
"s_waitcnt vmcnt(15)\n"
"ds_read_b128 a[192:195], v131 offset:0\n"
"ds_read_b128 a[196:199], v132 offset:0\n"
"s_add_u32 m0, s45, 35840\n"
"s_add_u32 s56, s48, 57344\n"
"buffer_load_dwordx4 v130, s[8:11], s56 offen lds\n"
"s_add_u32 s48, s48, 128\n"
"s_and_b32 s48, s48, 1023\n"
"v_mfma_f32_32x32x16_bf16 v[48:63], a[132:135], a[240:243], v[48:63]\n"
"ds_read_b64_tr_b16 a[162:163], v139 offset:41472\n"
"ds_read_b128 a[200:203], v133 offset:0\n"
"s_waitcnt lgkmcnt(8)\n"
"v_mfma_f32_32x32x16_bf16 v[64:79], a[136:139], a[224:227], v[64:79]\n"
"ds_read_b64_tr_b16 a[164:165], v140 offset:40960\n"
"ds_read_b128 a[204:207], v134 offset:0\n"
"s_add_u32 m0, s45, 147456\n"
"s_add_u32 s56, s49, 0\n"
"buffer_load_dwordx4 v128, s[12:15], s56 offen lds\n"
"v_mfma_f32_32x32x16_bf16 v[80:95], a[136:139], a[240:243], v[80:95]\n"
"ds_read_b64_tr_b16 a[166:167], v140 offset:41472\n"
"s_add_u32 m0, s45, 148480\n"
"s_add_u32 s56, s49, 128\n"
"buffer_load_dwordx4 v128, s[12:15], s56 offen lds\n"
"v_mfma_f32_32x32x16_bf16 v[96:111], a[140:143], a[224:227], v[96:111]\n"
"ds_read_b64_tr_b16 a[168:169], v139 offset:41984\n"
"v_mfma_f32_32x32x16_bf16 v[112:127], a[140:143], a[240:243], v[112:127]\n"
"ds_read_b64_tr_b16 a[170:171], v139 offset:42496\n"
"s_add_u32 m0, s45, 149504\n"
"s_add_u32 s56, s49, 65536\n"
"buffer_load_dwordx4 v128, s[12:15], s56 offen lds\n"
"v_mfma_f32_32x32x16_bf16 v[0:15], a[144:147], a[228:231], v[0:15]\n"
"ds_read_b64_tr_b16 a[172:173], v140 offset:41984\n"
"s_waitcnt lgkmcnt(4)\n"
"v_accvgpr_read_b32 v248, a207\n"
"v_mfma_f32_32x32x16_bf16 v[16:31], a[144:147], a[244:247], v[16:31]\n"
"s_add_u32 m0, s45, 150528\n"
"s_add_u32 s56, s49, 65664\n"
"buffer_load_dwordx4 v128, s[12:15], s56 offen lds\n"
"s_add_u32 s49, s49, 524288\n"
"s_and_b32 s49, s49, 4194303\n"
"v_mfma_f32_32x32x16_bf16 v[32:47], a[148:151], a[228:231], v[32:47]\n"
"ds_read_b64_tr_b16 a[174:175], v140 offset:42496\n"
"s_waitcnt vmcnt(16)\n"
"ds_read_b128 a[208:211], v135 offset:0\n"
"ds_read_b128 a[212:215], v136 offset:0\n"
"v_mfma_f32_32x32x16_bf16 v[48:63], a[148:151], a[244:247], v[48:63]\n"
"ds_read_b64_tr_b16 a[176:177], v139 offset:45056\n"
"ds_read_b128 a[216:219], v137 offset:0\n"
"s_add_u32 m0, s45, 49152\n"
"s_add_u32 s56, s48, 0\n"
"buffer_load_dwordx4 v129, s[8:11], s56 offen lds\n"
"v_mfma_f32_32x32x16_bf16 v[64:79], a[152:155], a[228:231], v[64:79]\n"
"ds_read_b64_tr_b16 a[178:179], v139 offset:45568\n"
"ds_read_b128 a[220:223], v138 offset:0\n"
"s_add_u32 m0, s45, 50176\n"
"s_add_u32 s56, s48, 8192\n"
"buffer_load_dwordx4 v130, s[8:11], s56 offen lds\n"
"v_mfma_f32_32x32x16_bf16 v[80:95], a[152:155], a[244:247], v[80:95]\n"
"ds_read_b64_tr_b16 a[180:181], v140 offset:45056\n"
"v_mfma_f32_32x32x16_bf16 v[96:111], a[156:159], a[228:231], v[96:111]\n"
"ds_read_b64_tr_b16 a[182:183], v140 offset:45568\n"
"s_add_u32 m0, s45, 51200\n"
"s_add_u32 s56, s48, 16384\n"
"buffer_load_dwordx4 v129, s[8:11], s56 offen lds\n"
"v_mfma_f32_32x32x16_bf16 v[112:127], a[156:159], a[244:247], v[112:127]\n"
"ds_read_b64_tr_b16 a[184:185], v139 offset:46080\n"
"v_mfma_f32_32x32x16_bf16 v[0:15], a[160:163], a[232:235], v[0:15]\n"
"ds_read_b64_tr_b16 a[186:187], v139 offset:46592\n"
"s_waitcnt lgkmcnt(4)\n"
"v_accvgpr_read_b32 v248, a223\n"
"s_add_u32 m0, s45, 52224\n"
"s_add_u32 s56, s48, 24576\n"
"buffer_load_dwordx4 v130, s[8:11], s56 offen lds\n"
"v_mfma_f32_32x32x16_bf16 v[16:31], a[160:163], a[248:251], v[16:31]\n"
"ds_read_b64_tr_b16 a[188:189], v140 offset:46080\n"
"v_mfma_f32_32x32x16_bf16 v[32:47], a[164:167], a[232:235], v[32:47]\n"
"ds_read_b64_tr_b16 a[190:191], v140 offset:46592\n"
"s_add_u32 m0, s45, 114688\n"
"s_add_u32 s56, s48, 32768\n"
"buffer_load_dwordx4 v129, s[8:11], s56 offen lds\n"
"v_mfma_f32_32x32x16_bf16 v[48:63], a[164:167], a[248:251], v[48:63]\n"
"v_mfma_f32_32x32x16_bf16 v[64:79], a[168:171], a[232:235], v[64:79]\n"
"s_add_u32 m0, s45, 115712\n"
"s_add_u32 s56, s48, 40960\n"
"buffer_load_dwordx4 v130, s[8:11], s56 offen lds\n"
"v_mfma_f32_32x32x16_bf16 v[80:95], a[168:171], a[248:251], v[80:95]\n"
"s_add_u32 m0, s45, 116736\n"
"s_add_u32 s56, s48, 49152\n"
"buffer_load_dwordx4 v129, s[8:11], s56 offen lds\n"
"v_mfma_f32_32x32x16_bf16 v[96:111], a[172:175], a[232:235], v[96:111]\n"
"v_mfma_f32_32x32x16_bf16 v[112:127], a[172:175], a[248:251], v[112:127]\n"
"s_add_u32 m0, s45, 117760\n"
"s_add_u32 s56, s48, 57344\n"
"buffer_load_dwordx4 v130, s[8:11], s56 offen lds\n"
"s_add_u32 s48, s48, 128\n"
"s_and_b32 s48, s48, 1023\n"
"v_mfma_f32_32x32x16_bf16 v[0:15], a[176:179], a[236:239], v[0:15]\n"
"s_waitcnt vmcnt(20)\n"
"s_waitcnt lgkmcnt(0)\n"
"s_barrier\n"
"ds_read_b64_tr_b16 a[128:129], v141 offset:0\n"
"ds_read_b64_tr_b16 a[130:131], v141 offset:512\n"
"v_mfma_f32_32x32x16_bf16 v[16:31], a[176:179], a[252:255], v[16:31]\n"
"ds_read_b64_tr_b16 a[132:133], v142 offset:0\n"
"ds_read_b64_tr_b16 a[134:135], v142 offset:512\n"
"s_add_u32 m0, s45, 16384\n"
"s_add_u32 s56, s49, 0\n"
"buffer_load_dwordx4 v128, s[12:15], s56 offen lds\n"
"v_mfma_f32_32x32x16_bf16 v[32:47], a[180:183], a[236:239], v[32:47]\n"
"ds_read_b64_tr_b16 a[136:137], v141 offset:1024\n"
"ds_read_b64_tr_b16 a[138:139], v141 offset:1536\n"
"v_mfma_f32_32x32x16_bf16 v[48:63], a[180:183], a[252:255], v[48:63]\n"
"ds_read_b64_tr_b16 a[140:141], v142 offset:1024\n"
"s_add_u32 m0, s45, 17408\n"
"s_add_u32 s56, s49, 128\n"
"buffer_load_dwordx4 v128, s[12:15], s56 offen lds\n"
"v_mfma_f32_32x32x16_bf16 v[64:79], a[184:187], a[236:239], v[64:79]\n"
"ds_read_b64_tr_b16 a[142:143], v142 offset:1536\n"
"ds_read_b64_tr_b16 a[144:145], v141 offset:4096\n"
"v_mfma_f32_32x32x16_bf16 v[80:95], a[184:187], a[252:255], v[80:95]\n"
"ds_read_b64_tr_b16 a[146:147], v141 offset:4608\n"
"ds_read_b64_tr_b16 a[148:149], v142 offset:4096\n"
"s_add_u32 m0, s45, 18432\n"
"s_add_u32 s56, s49, 65536\n"
"buffer_load_dwordx4 v128, s[12:15], s56 offen lds\n"
"v_mfma_f32_32x32x16_bf16 v[96:111], a[188:191], a[236:239], v[96:111]\n"
"ds_read_b64_tr_b16 a[150:151], v142 offset:4608\n"
"ds_read_b64_tr_b16 a[152:153], v141 offset:5120\n"
"s_add_u32 m0, s45, 19456\n"
"s_add_u32 s56, s49, 65664\n"
"buffer_load_dwordx4 v128, s[12:15], s56 offen lds\n"
"s_add_u32 s49, s49, 524288\n"
"s_and_b32 s49, s49, 4194303\n"
"v_mfma_f32_32x32x16_bf16 v[112:127], a[188:191], a[252:255], v[112:127]\n"
"ds_read_b64_tr_b16 a[154:155], v141 offset:5632\n"
"s_waitcnt lgkmcnt(8)\n"
"v_mfma_f32_32x32x16_bf16 v[0:15], a[128:131], a[192:195], v[0:15]\n"
"ds_read_b64_tr_b16 a[156:157], v142 offset:5120\n"
"s_add_u32 m0, s45, 0\n"
"s_add_u32 s56, s48, 0\n"
"buffer_load_dwordx4 v129, s[8:11], s56 offen lds\n"
"v_mfma_f32_32x32x16_bf16 v[16:31], a[128:131], a[208:211], v[16:31]\n"
"ds_read_b64_tr_b16 a[158:159], v142 offset:5632\n"
"v_mfma_f32_32x32x16_bf16 v[32:47], a[132:135], a[192:195], v[32:47]\n"
"ds_read_b64_tr_b16 a[160:161], v141 offset:8192\n"
"s_waitcnt vmcnt(21)\n"
"ds_read_b128 a[224:227], v135 offset:16384\n"
"ds_read_b128 a[228:231], v136 offset:16384\n"
"s_add_u32 m0, s45, 1024\n"
"s_add_u32 s56, s48, 8192\n"
"buffer_load_dwordx4 v130, s[8:11], s56 offen lds\n"
"v_mfma_f32_32x32x16_bf16 v[48:63], a[132:135], a[208:211], v[48:63]\n"
"ds_read_b64_tr_b16 a[162:163], v141 offset:8704\n"
"ds_read_b128 a[232:235], v137 offset:16384\n"
"v_mfma_f32_32x32x16_bf16 v[64:79], a[136:139], a[192:195], v[64:79]\n"
"ds_read_b64_tr_b16 a[164:165], v142 offset:8192\n"
"ds_read_b128 a[236:239], v138 offset:16384\n"
"s_add_u32 m0, s45, 2048\n"
"s_add_u32 s56, s48, 16384\n"
"buffer_load_dwordx4 v129, s[8:11], s56 offen lds\n"
"v_mfma_f32_32x32x16_bf16 v[80:95], a[136:139], a[208:211], v[80:95]\n"
"ds_read_b64_tr_b16 a[166:167], v142 offset:8704\n"
"s_waitcnt lgkmcnt(9)\n"
"v_mfma_f32_32x32x16_bf16 v[96:111], a[140:143], a[192:195], v[96:111]\n"
"ds_read_b64_tr_b16 a[168:169], v141 offset:9216\n"
"s_add_u32 m0, s45, 3072\n"
"s_add_u32 s56, s48, 24576\n"
"buffer_load_dwordx4 v130, s[8:11], s56 offen lds\n"
"v_mfma_f32_32x32x16_bf16 v[112:127], a[140:143], a[208:211], v[112:127]\n"
"ds_read_b64_tr_b16 a[170:171], v141 offset:9728\n"
"s_add_u32 m0, s45, 65536\n"
"s_add_u32 s56, s48, 32768\n"
"buffer_load_dwordx4 v129, s[8:11], s56 offen lds\n"
"v_mfma_f32_32x32x16_bf16 v[0:15], a[144:147], a[196:199], v[0:15]\n"
"ds_read_b64_tr_b16 a[172:173], v142 offset:9216\n"
"s_waitcnt lgkmcnt(4)\n"
"v_accvgpr_read_b32 v248, a239\n"
"v_mfma_f32_32x32x16_bf16 v[16:31], a[144:147], a[212:215], v[16:31]\n"
"s_add_u32 m0, s45, 66560\n"
"s_add_u32 s56, s48, 40960\n"
"buffer_load_dwordx4 v130, s[8:11], s56 offen lds\n"
"v_mfma_f32_32x32x16_bf16 v[32:47], a[148:151], a[196:199], v[32:47]\n"
"ds_read_b64_tr_b16 a[174:175], v142 offset:9728\n"
"s_waitcnt vmcnt(22)\n"
"ds_read_b128 a[240:243], v131 offset:32768\n"
"ds_read_b128 a[244:247], v132 offset:32768\n"
"v_mfma_f32_32x32x16_bf16 v[48:63], a[148:151], a[212:215], v[48:63]\n"
"ds_read_b64_tr_b16 a[176:177], v141 offset:12288\n"
"ds_read_b128 a[248:251], v133 offset:32768\n"
"s_add_u32 m0, s45, 67584\n"
"s_add_u32 s56, s48, 49152\n"
"buffer_load_dwordx4 v129, s[8:11], s56 offen lds\n"
"v_mfma_f32_32x32x16_bf16 v[64:79], a[152:155], a[196:199], v[64:79]\n"
"ds_read_b64_tr_b16 a[178:179], v141 offset:12800\n"
"ds_read_b128 a[252:255], v134 offset:32768\n"
"v_mfma_f32_32x32x16_bf16 v[80:95], a[152:155], a[212:215], v[80:95]\n"
"ds_read_b64_tr_b16 a[180:181], v142 offset:12288\n"
"s_add_u32 m0, s45, 68608\n"
"s_add_u32 s56, s48, 57344\n"
"buffer_load_dwordx4 v130, s[8:11], s56 offen lds\n"
"s_add_u32 s48, s48, 128\n"
"s_and_b32 s48, s48, 1023\n"
"v_mfma_f32_32x32x16_bf16 v[96:111], a[156:159], a[196:199], v[96:111]\n"
"ds_read_b64_tr_b16 a[182:183], v142 offset:12800\n"
"v_mfma_f32_32x32x16_bf16 v[112:127], a[156:159], a[212:215], v[112:127]\n"
"ds_read_b64_tr_b16 a[184:185], v141 offset:13312\n"
"s_add_u32 m0, s45, 98304\n"
"s_add_u32 s56, s49, 0\n"
"buffer_load_dwordx4 v128, s[12:15], s56 offen lds\n"
"v_mfma_f32_32x32x16_bf16 v[0:15], a[160:163], a[200:203], v[0:15]\n"
"ds_read_b64_tr_b16 a[186:187], v141 offset:13824\n"
"s_waitcnt lgkmcnt(4)\n"
"v_accvgpr_read_b32 v248, a255\n"
"s_add_u32 m0, s45, 99328\n"
"s_add_u32 s56, s49, 128\n"
"buffer_load_dwordx4 v128, s[12:15], s56 offen lds\n"
"v_mfma_f32_32x32x16_bf16 v[16:31], a[160:163], a[216:219], v[16:31]\n"
"ds_read_b64_tr_b16 a[188:189], v142 offset:13312\n"
"v_mfma_f32_32x32x16_bf16 v[32:47], a[164:167], a[200:203], v[32:47]\n"
"ds_read_b64_tr_b16 a[190:191], v142 offset:13824\n"
"s_add_u32 m0, s45, 100352\n"
"s_add_u32 s56, s49, 65536\n"
"buffer_load_dwordx4 v128, s[12:15], s56 offen lds\n"
"v_mfma_f32_32x32x16_bf16 v[48:63], a[164:167], a[216:219], v[48:63]\n"
"v_mfma_f32_32x32x16_bf16 v[64:79], a[168:171], a[200:203], v[64:79]\n"
"s_add_u32 m0, s45, 101376\n"
"s_add_u32 s56, s49, 65664\n"
"buffer_load_dwordx4 v128, s[12:15], s56 offen lds\n"
"s_add_u32 s49, s49, 524288\n"
"s_and_b32 s49, s49, 4194303\n"
"v_mfma_f32_32x32x16_bf16 v[80:95], a[168:171], a[216:219], v[80:95]\n"
"v_mfma_f32_32x32x16_bf16 v[96:111], a[172:175], a[200:203], v[96:111]\n"
"s_add_u32 m0, s45, 81920\n"
"s_add_u32 s56, s48, 0\n"
"buffer_load_dwordx4 v129, s[8:11], s56 offen lds\n"
"v_mfma_f32_32x32x16_bf16 v[112:127], a[172:175], a[216:219], v[112:127]\n"
"v_mfma_f32_32x32x16_bf16 v[0:15], a[176:179], a[204:207], v[0:15]\n"
"s_waitcnt vmcnt(25)\n"
"s_waitcnt lgkmcnt(0)\n"
"s_barrier\n"
"ds_read_b64_tr_b16 a[128:129], v141 offset:16384\n"
"ds_read_b64_tr_b16 a[130:131], v141 offset:16896\n"
"s_add_u32 m0, s45, 82944\n"
"s_add_u32 s56, s48, 8192\n"
"buffer_load_dwordx4 v130, s[8:11], s56 offen lds\n"
"v_mfma_f32_32x32x16_bf16 v[16:31], a[176:179], a[220:223], v[16:31]\n"
"ds_read_b64_tr_b16 a[132:133], v142 offset:16384\n"
"ds_read_b64_tr_b16 a[134:135], v142 offset:16896\n"
"s_add_u32 m0, s45, 83968\n"
"s_add_u32 s56, s48, 16384\n"
"buffer_load_dwordx4 v129, s[8:11], s56 offen lds\n"
"v_mfma_f32_32x32x16_bf16 v[32:47], a[180:183], a[204:207], v[32:47]\n"
"ds_read_b64_tr_b16 a[136:137], v141 offset:17408\n"
"ds_read_b64_tr_b16 a[138:139], v141 offset:17920\n"
"v_mfma_f32_32x32x16_bf16 v[48:63], a[180:183], a[220:223], v[48:63]\n"
"ds_read_b64_tr_b16 a[140:141], v142 offset:17408\n"
"s_add_u32 m0, s45, 84992\n"
"s_add_u32 s56, s48, 24576\n"
"buffer_load_dwordx4 v130, s[8:11], s56 offen lds\n"
"v_mfma_f32_32x32x16_bf16 v[64:79], a[184:187], a[204:207], v[64:79]\n"
"ds_read_b64_tr_b16 a[142:143], v142 offset:17920\n"
"ds_read_b64_tr_b16 a[144:145], v141 offset:20480\n"
"v_mfma_f32_32x32x16_bf16 v[80:95], a[184:187], a[220:223], v[80:95]\n"
"ds_read_b64_tr_b16 a[146:147], v141 offset:20992\n"
"ds_read_b64_tr_b16 a[148:149], v142 offset:20480\n"
"s_add_u32 m0, s45, 32768\n"
"s_add_u32 s56, s48, 32768\n"
"buffer_load_dwordx4 v129, s[8:11], s56 offen lds\n"
"v_mfma_f32_32x32x16_bf16 v[96:111], a[188:191], a[204:207], v[96:111]\n"
"ds_read_b64_tr_b16 a[150:151], v142 offset:20992\n"
"ds_read_b64_tr_b16 a[152:153], v141 offset:21504\n"
"v_mfma_f32_32x32x16_bf16 v[112:127], a[188:191], a[220:223], v[112:127]\n"
"ds_read_b64_tr_b16 a[154:155], v141 offset:22016\n"
"s_add_u32 m0, s45, 33792\n"
"s_add_u32 s56, s48, 40960\n"
"buffer_load_dwordx4 v130, s[8:11], s56 offen lds\n"
"s_waitcnt lgkmcnt(9)\n"
"v_mfma_f32_32x32x16_bf16 v[0:15], a[128:131], a[224:227], v[0:15]\n"
"ds_read_b64_tr_b16 a[156:157], v142 offset:21504\n"
"v_mfma_f32_32x32x16_bf16 v[16:31], a[128:131], a[240:243], v[16:31]\n"
"ds_read_b64_tr_b16 a[158:159], v142 offset:22016\n"
"s_add_u32 m0, s45, 34816\n"
"s_add_u32 s56, s48, 49152\n"
"buffer_load_dwordx4 v129, s[8:11], s56 offen lds\n"
"v_mfma_f32_32x32x16_bf16 v[32:47], a[132:135], a[224:227], v[32:47]\n"
"ds_read_b64_tr_b16 a[160:161], v141 offset:24576\n"
"s_waitcnt vmcnt(27)\n"
"ds_read_b128 a[192:195], v131 offset:49152\n"
"ds_read_b128 a[196:199], v132 offset:49152\n"
"s_add_u32 m0, s45, 35840\n"
"s_add_u32 s56, s48, 57344\n"
"buffer_load_dwordx4 v130, s[8:11], s56 offen lds\n"
"s_add_u32 s48, s48, 128\n"
"s_and_b32 s48, s48, 1023\n"
"v_mfma_f32_32x32x16_bf16 v[48:63], a[132:135], a[240:243], v[48:63]\n"
"ds_read_b64_tr_b16 a[162:163], v141 offset:25088\n"
"ds_read_b128 a[200:203], v133 offset:49152\n"
"s_waitcnt lgkmcnt(7)\n"
"v_mfma_f32_32x32x16_bf16 v[64:79], a[136:139], a[224:227], v[64:79]\n"
"ds_read_b64_tr_b16 a[164:165], v142 offset:24576\n"
"ds_read_b128 a[204:207], v134 offset:49152\n"
"s_add_u32 m0, s45, 131072\n"
"s_add_u32 s56, s49, 0\n"
"buffer_load_dwordx4 v128, s[12:15], s56 offen lds\n"
"v_mfma_f32_32x32x16_bf16 v[80:95], a[136:139], a[240:243], v[80:95]\n"
"ds_read_b64_tr_b16 a[166:167], v142 offset:25088\n"
"v_mfma_f32_32x32x16_bf16 v[96:111], a[140:143], a[224:227], v[96:111]\n"
"ds_read_b64_tr_b16 a[168:169], v141 offset:25600\n"
"s_add_u32 m0, s45, 132096\n"
"s_add_u32 s56, s49, 128\n"
"buffer_load_dwordx4 v128, s[12:15], s56 offen lds\n"
"v_mfma_f32_32x32x16_bf16 v[112:127], a[140:143], a[240:243], v[112:127]\n"
"ds_read_b64_tr_b16 a[170:171], v141 offset:26112\n"
"v_mfma_f32_32x32x16_bf16 v[0:15], a[144:147], a[228:231], v[0:15]\n"
"ds_read_b64_tr_b16 a[172:173], v142 offset:25600\n"
"s_waitcnt lgkmcnt(4)\n"
"v_accvgpr_read_b32 v248, a207\n"
"s_add_u32 m0, s45, 133120\n"
"s_add_u32 s56, s49, 65536\n"
"buffer_load_dwordx4 v128, s[12:15], s56 offen lds\n"
"v_mfma_f32_32x32x16_bf16 v[16:31], a[144:147], a[244:247], v[16:31]\n"
"v_mfma_f32_32x32x16_bf16 v[32:47], a[148:151], a[228:231], v[32:47]\n"
"ds_read_b64_tr_b16 a[174:175], v142 offset:26112\n"
"s_waitcnt vmcnt(27)\n"
"ds_read_b128 a[208:211], v135 offset:49152\n"
"ds_read_b128 a[212:215], v136 offset:49152\n"
"s_add_u32 m0, s45, 134144\n"
"s_add_u32 s56, s49, 65664\n"
"buffer_load_dwordx4 v128, s[12:15], s56 offen lds\n"
"s_add_u32 s49, s49, 524288\n"
"s_and_b32 s49, s49, 4194303\n"
"v_mfma_f32_32x32x16_bf16 v[48:63], a[148:151], a[244:247], v[48:63]\n"
"ds_read_b64_tr_b16 a[176:177], v141 offset:28672\n"
"ds_read_b128 a[216:219], v137 offset:49152\n"
"s_add_u32 m0, s45, 49152\n"
"s_add_u32 s56, s48, 0\n"
"buffer_load_dwordx4 v129, s[8:11], s56 offen lds\n"
"v_mfma_f32_32x32x16_bf16 v[64:79], a[152:155], a[228:231], v[64:79]\n"
"ds_read_b64_tr_b16 a[178:179], v141 offset:29184\n"
"ds_read_b128 a[220:223], v138 offset:49152\n"
"v_mfma_f32_32x32x16_bf16 v[80:95], a[152:155], a[244:247], v[80:95]\n"
"ds_read_b64_tr_b16 a[180:181], v142 offset:28672\n"
"s_add_u32 m0, s45, 50176\n"
"s_add_u32 s56, s48, 8192\n"
"buffer_load_dwordx4 v130, s[8:11], s56 offen lds\n"
"v_mfma_f32_32x32x16_bf16 v[96:111], a[156:159], a[228:231], v[96:111]\n"
"ds_read_b64_tr_b16 a[182:183], v142 offset:29184\n"
"v_mfma_f32_32x32x16_bf16 v[112:127], a[156:159], a[244:247], v[112:127]\n"
"ds_read_b64_tr_b16 a[184:185], v141 offset:29696\n"
"s_add_u32 m0, s45, 51200\n"
"s_add_u32 s56, s48, 16384\n"
"buffer_load_dwordx4 v129, s[8:11], s56 offen lds\n"
"v_mfma_f32_32x32x16_bf16 v[0:15], a[160:163], a[232:235], v[0:15]\n"
"ds_read_b64_tr_b16 a[186:187], v141 offset:30208\n"
"s_waitcnt lgkmcnt(4)\n"
"v_accvgpr_read_b32 v248, a223\n"
"v_mfma_f32_32x32x16_bf16 v[16:31], a[160:163], a[248:251], v[16:31]\n"
"ds_read_b64_tr_b16 a[188:189], v142 offset:29696\n"
"s_add_u32 m0, s45, 52224\n"
"s_add_u32 s56, s48, 24576\n"
"buffer_load_dwordx4 v130, s[8:11], s56 offen lds\n"
"v_mfma_f32_32x32x16_bf16 v[32:47], a[164:167], a[232:235], v[32:47]\n"
"ds_read_b64_tr_b16 a[190:191], v142 offset:30208\n"
"v_mfma_f32_32x32x16_bf16 v[48:63], a[164:167], a[248:251], v[48:63]\n"
"s_add_u32 m0, s45, 114688\n"
"s_add_u32 s56, s48, 32768\n"
"buffer_load_dwordx4 v129, s[8:11], s56 offen lds\n"
"v_mfma_f32_32x32x16_bf16 v[64:79], a[168:171], a[232:235], v[64:79]\n"
"s_add_u32 m0, s45, 115712\n"
"s_add_u32 s56, s48, 40960\n"
"buffer_load_dwordx4 v130, s[8:11], s56 offen lds\n"
"v_mfma_f32_32x32x16_bf16 v[80:95], a[168:171], a[248:251], v[80:95]\n"
"v_mfma_f32_32x32x16_bf16 v[96:111], a[172:175], a[232:235], v[96:111]\n"
"s_add_u32 m0, s45, 116736\n"
"s_add_u32 s56, s48, 49152\n"
"buffer_load_dwordx4 v129, s[8:11], s56 offen lds\n"
"v_mfma_f32_32x32x16_bf16 v[112:127], a[172:175], a[248:251], v[112:127]\n"
"v_mfma_f32_32x32x16_bf16 v[0:15], a[176:179], a[236:239], v[0:15]\n"
"s_waitcnt vmcnt(31)\n"
"s_waitcnt lgkmcnt(0)\n"
"s_barrier\n"
"ds_read_b64_tr_b16 a[128:129], v143 offset:16384\n"
"ds_read_b64_tr_b16 a[130:131], v143 offset:16896\n"
"s_add_u32 m0, s45, 117760\n"
"s_add_u32 s56, s48, 57344\n"
"buffer_load_dwordx4 v130, s[8:11], s56 offen lds\n"
"s_add_u32 s48, s48, 128\n"
"s_and_b32 s48, s48, 1023\n"
"v_mfma_f32_32x32x16_bf16 v[16:31], a[176:179], a[252:255], v[16:31]\n"
"ds_read_b64_tr_b16 a[132:133], v144 offset:16384\n"
"ds_read_b64_tr_b16 a[134:135], v144 offset:16896\n"
"v_mfma_f32_32x32x16_bf16 v[32:47], a[180:183], a[236:239], v[32:47]\n"
"ds_read_b64_tr_b16 a[136:137], v143 offset:17408\n"
"ds_read_b64_tr_b16 a[138:139], v143 offset:17920\n"
"s_add_u32 m0, s45, 147456\n"
"s_add_u32 s56, s49, 0\n"
"buffer_load_dwordx4 v128, s[12:15], s56 offen lds\n"
"v_mfma_f32_32x32x16_bf16 v[48:63], a[180:183], a[252:255], v[48:63]\n"
"ds_read_b64_tr_b16 a[140:141], v144 offset:17408\n"
"v_mfma_f32_32x32x16_bf16 v[64:79], a[184:187], a[236:239], v[64:79]\n"
"ds_read_b64_tr_b16 a[142:143], v144 offset:17920\n"
"ds_read_b64_tr_b16 a[144:145], v143 offset:20480\n"
"s_add_u32 m0, s45, 148480\n"
"s_add_u32 s56, s49, 128\n"
"buffer_load_dwordx4 v128, s[12:15], s56 offen lds\n"
"v_mfma_f32_32x32x16_bf16 v[80:95], a[184:187], a[252:255], v[80:95]\n"
"ds_read_b64_tr_b16 a[146:147], v143 offset:20992\n"
"ds_read_b64_tr_b16 a[148:149], v144 offset:20480\n"
"s_add_u32 m0, s45, 149504\n"
"s_add_u32 s56, s49, 65536\n"
"buffer_load_dwordx4 v128, s[12:15], s56 offen lds\n"
"v_mfma_f32_32x32x16_bf16 v[96:111], a[188:191], a[236:239], v[96:111]\n"
"ds_read_b64_tr_b16 a[150:151], v144 offset:20992\n"
"ds_read_b64_tr_b16 a[152:153], v143 offset:21504\n"
"v_mfma_f32_32x32x16_bf16 v[112:127], a[188:191], a[252:255], v[112:127]\n"
"ds_read_b64_tr_b16 a[154:155], v143 offset:22016\n"
"s_add_u32 m0, s45, 150528\n"
"s_add_u32 s56, s49, 65664\n"
"buffer_load_dwordx4 v128, s[12:15], s56 offen lds\n"
"s_add_u32 s49, s49, 524288\n"
"s_and_b32 s49, s49, 4194303\n"
"s_waitcnt lgkmcnt(8)\n"
"v_mfma_f32_32x32x16_bf16 v[0:15], a[128:131], a[192:195], v[0:15]\n"
"ds_read_b64_tr_b16 a[156:157], v144 offset:21504\n"
"v_mfma_f32_32x32x16_bf16 v[16:31], a[128:131], a[208:211], v[16:31]\n"
"ds_read_b64_tr_b16 a[158:159], v144 offset:22016\n"
"v_mfma_f32_32x32x16_bf16 v[32:47], a[132:135], a[192:195], v[32:47]\n"
"ds_read_b64_tr_b16 a[160:161], v143 offset:24576\n"
"s_waitcnt vmcnt(32)\n"
"ds_read_b128 a[224:227], v131 offset:0\n"
"ds_read_b128 a[228:231], v132 offset:0\n"
"v_mfma_f32_32x32x16_bf16 v[48:63], a[132:135], a[208:211], v[48:63]\n"
"ds_read_b64_tr_b16 a[162:163], v143 offset:25088\n"
"ds_read_b128 a[232:235], v133 offset:0\n"
"v_mfma_f32_32x32x16_bf16 v[64:79], a[136:139], a[192:195], v[64:79]\n"
"ds_read_b64_tr_b16 a[164:165], v144 offset:24576\n"
"ds_read_b128 a[236:239], v134 offset:0\n"
"v_mfma_f32_32x32x16_bf16 v[80:95], a[136:139], a[208:211], v[80:95]\n"
"ds_read_b64_tr_b16 a[166:167], v144 offset:25088\n"
"s_waitcnt lgkmcnt(10)\n"
"v_mfma_f32_32x32x16_bf16 v[96:111], a[140:143], a[192:195], v[96:111]\n"
"ds_read_b64_tr_b16 a[168:169], v143 offset:25600\n"
"v_mfma_f32_32x32x16_bf16 v[112:127], a[140:143], a[208:211], v[112:127]\n"
"ds_read_b64_tr_b16 a[170:171], v143 offset:26112\n"
"v_mfma_f32_32x32x16_bf16 v[0:15], a[144:147], a[196:199], v[0:15]\n"
"ds_read_b64_tr_b16 a[172:173], v144 offset:25600\n"
"s_waitcnt lgkmcnt(4)\n"
"v_accvgpr_read_b32 v248, a239\n"
"v_mfma_f32_32x32x16_bf16 v[16:31], a[144:147], a[212:215], v[16:31]\n"
"s_add_u32 m0, s45, 0\n"
"s_xor_b32 s56, s86, 0\n"
"buffer_load_dwordx4 v224, s[16:19], s56 offen nt lds\n"
"v_mfma_f32_32x32x16_bf16 v[32:47], a[148:151], a[196:199], v[32:47]\n"
"ds_read_b64_tr_b16 a[174:175], v144 offset:26112\n"
"s_waitcnt vmcnt(29)\n"
"ds_read_b128 a[240:243], v135 offset:0\n"
"ds_read_b128 a[244:247], v136 offset:0\n"
"s_mov_b64 exec, s[46:47]\n"
"buffer_load_dword v247, v245, s[40:43], s59 offen sc1\n"
"s_mov_b64 exec, -1\n"
"v_mfma_f32_32x32x16_bf16 v[48:63], a[148:151], a[212:215], v[48:63]\n"
"ds_read_b64_tr_b16 a[176:177], v143 offset:28672\n"
"ds_read_b128 a[248:251], v137 offset:0\n"
"v_mfma_f32_32x32x16_bf16 v[64:79], a[152:155], a[196:199], v[64:79]\n"
"ds_read_b64_tr_b16 a[178:179], v143 offset:29184\n"
"ds_read_b128 a[252:255], v138 offset:0\n"
"v_mfma_f32_32x32x16_bf16 v[80:95], a[152:155], a[212:215], v[80:95]\n"
"ds_read_b64_tr_b16 a[180:181], v144 offset:28672\n"
"v_mfma_f32_32x32x16_bf16 v[96:111], a[156:159], a[196:199], v[96:111]\n"
"ds_read_b64_tr_b16 a[182:183], v144 offset:29184\n"
"s_xor_b32 s56, s86, 0\n"
"buffer_load_dwordx4 v225, s[16:19], s56 offen offset:2048 nt lds\n"
"v_mfma_f32_32x32x16_bf16 v[112:127], a[156:159], a[212:215], v[112:127]\n"
"ds_read_b64_tr_b16 a[184:185], v143 offset:29696\n"
"v_mfma_f32_32x32x16_bf16 v[0:15], a[160:163], a[200:203], v[0:15]\n"
"ds_read_b64_tr_b16 a[186:187], v143 offset:30208\n"
"s_waitcnt lgkmcnt(4)\n"
"v_accvgpr_read_b32 v248, a255\n"
"v_mfma_f32_32x32x16_bf16 v[16:31], a[160:163], a[216:219], v[16:31]\n"
"ds_read_b64_tr_b16 a[188:189], v144 offset:29696\n"
"v_mfma_f32_32x32x16_bf16 v[32:47], a[164:167], a[200:203], v[32:47]\n"
"ds_read_b64_tr_b16 a[190:191], v144 offset:30208\n"
"v_mfma_f32_32x32x16_bf16 v[48:63], a[164:167], a[216:219], v[48:63]\n"
"s_add_u32 m0, s45, 1024\n"
"s_xor_b32 s56, s86, 128\n"
"buffer_load_dwordx4 v224, s[16:19], s56 offen nt lds\n"
"v_mfma_f32_32x32x16_bf16 v[64:79], a[168:171], a[200:203], v[64:79]\n"
"v_mfma_f32_32x32x16_bf16 v[80:95], a[168:171], a[216:219], v[80:95]\n"
"v_mfma_f32_32x32x16_bf16 v[96:111], a[172:175], a[200:203], v[96:111]\n"
"v_mfma_f32_32x32x16_bf16 v[112:127], a[172:175], a[216:219], v[112:127]\n"
"v_mfma_f32_32x32x16_bf16 v[0:15], a[176:179], a[204:207], v[0:15]\n"
"s_waitcnt vmcnt(28)\n"
"s_waitcnt lgkmcnt(0)\n"
"s_barrier\n"
"ds_read_b64_tr_b16 a[128:129], v139 offset:32768\n"
"ds_read_b64_tr_b16 a[130:131], v139 offset:33280\n"
"s_xor_b32 s56, s86, 128\n"
"buffer_load_dwordx4 v225, s[16:19], s56 offen offset:2048 nt lds\n"
"v_mfma_f32_32x32x16_bf16 v[16:31], a[176:179], a[220:223], v[16:31]\n"
"ds_read_b64_tr_b16 a[132:133], v140 offset:32768\n"
"ds_read_b64_tr_b16 a[134:135], v140 offset:33280\n"
"v_mfma_f32_32x32x16_bf16 v[32:47], a[180:183], a[204:207], v[32:47]\n"
"ds_read_b64_tr_b16 a[136:137], v139 offset:33792\n"
"ds_read_b64_tr_b16 a[138:139], v139 offset:34304\n"
"v_mfma_f32_32x32x16_bf16 v[48:63], a[180:183], a[220:223], v[48:63]\n"
"ds_read_b64_tr_b16 a[140:141], v140 offset:33792\n"
"v_mfma_f32_32x32x16_bf16 v[64:79], a[184:187], a[204:207], v[64:79]\n"
"ds_read_b64_tr_b16 a[142:143], v140 offset:34304\n"
"ds_read_b64_tr_b16 a[144:145], v139 offset:36864\n"
"v_mfma_f32_32x32x16_bf16 v[80:95], a[184:187], a[220:223], v[80:95]\n"
"ds_read_b64_tr_b16 a[146:147], v139 offset:37376\n"
"ds_read_b64_tr_b16 a[148:149], v140 offset:36864\n"
"s_add_u32 m0, s45, 65536\n"
"s_xor_b32 s56, s86, 0\n"
"buffer_load_dwordx4 v226, s[20:23], s56 offen nt lds\n"
"v_mfma_f32_32x32x16_bf16 v[96:111], a[188:191], a[204:207], v[96:111]\n"
"ds_read_b64_tr_b16 a[150:151], v140 offset:37376\n"
"ds_read_b64_tr_b16 a[152:153], v139 offset:37888\n"
"v_mfma_f32_32x32x16_bf16 v[112:127], a[188:191], a[220:223], v[112:127]\n"
"ds_read_b64_tr_b16 a[154:155], v139 offset:38400\n"
"s_waitcnt lgkmcnt(12)\n"
"v_mfma_f32_32x32x16_bf16 v[0:15], a[128:131], a[224:227], v[0:15]\n"
"ds_read_b64_tr_b16 a[156:157], v140 offset:37888\n"
"v_mfma_f32_32x32x16_bf16 v[16:31], a[128:131], a[240:243], v[16:31]\n"
"ds_read_b64_tr_b16 a[158:159], v140 offset:38400\n"
"s_waitcnt lgkmcnt(12)\n"
"v_mfma_f32_32x32x16_bf16 v[32:47], a[132:135], a[224:227], v[32:47]\n"
"ds_read_b64_tr_b16 a[160:161], v139 offset:40960\n"
"s_waitcnt vmcnt(26)\n"
"ds_read_b128 a[192:195], v135 offset:16384\n"
"ds_read_b128 a[196:199], v136 offset:16384\n"
"s_xor_b32 s56, s86, 0\n"
"buffer_load_dwordx4 v226, s[20:23], s56 offen offset:2048 nt lds\n"
"v_mfma_f32_32x32x16_bf16 v[48:63], a[132:135], a[240:243], v[48:63]\n"
"ds_read_b64_tr_b16 a[162:163], v139 offset:41472\n"
"ds_read_b128 a[200:203], v137 offset:16384\n"
"s_waitcnt lgkmcnt(10)\n"
"v_mfma_f32_32x32x16_bf16 v[64:79], a[136:139], a[224:227], v[64:79]\n"
"ds_read_b64_tr_b16 a[164:165], v140 offset:40960\n"
"ds_read_b128 a[204:207], v138 offset:16384\n"
"v_mfma_f32_32x32x16_bf16 v[80:95], a[136:139], a[240:243], v[80:95]\n"
"ds_read_b64_tr_b16 a[166:167], v140 offset:41472\n"
"v_mfma_f32_32x32x16_bf16 v[96:111], a[140:143], a[224:227], v[96:111]\n"
"ds_read_b64_tr_b16 a[168:169], v139 offset:41984\n"
"v_mfma_f32_32x32x16_bf16 v[112:127], a[140:143], a[240:243], v[112:127]\n"
"ds_read_b64_tr_b16 a[170:171], v139 offset:42496\n"
"s_add_u32 m0, s45, 66560\n"
"s_xor_b32 s56, s86, 128\n"
"buffer_load_dwordx4 v226, s[20:23], s56 offen nt lds\n"
"v_mfma_f32_32x32x16_bf16 v[0:15], a[144:147], a[228:231], v[0:15]\n"
"ds_read_b64_tr_b16 a[172:173], v140 offset:41984\n"
"s_waitcnt lgkmcnt(4)\n"
"v_accvgpr_read_b32 v248, a207\n"
"v_mfma_f32_32x32x16_bf16 v[16:31], a[144:147], a[244:247], v[16:31]\n"
"v_mfma_f32_32x32x16_bf16 v[32:47], a[148:151], a[228:231], v[32:47]\n"
"ds_read_b64_tr_b16 a[174:175], v140 offset:42496\n"
"s_waitcnt vmcnt(24)\n"
"ds_read_b128 a[208:211], v131 offset:32768\n"
"ds_read_b128 a[212:215], v132 offset:32768\n"
"v_mfma_f32_32x32x16_bf16 v[48:63], a[148:151], a[244:247], v[48:63]\n"
"ds_read_b64_tr_b16 a[176:177], v139 offset:45056\n"
"ds_read_b128 a[216:219], v133 offset:32768\n"
"v_mfma_f32_32x32x16_bf16 v[64:79], a[152:155], a[228:231], v[64:79]\n"
"ds_read_b64_tr_b16 a[178:179], v139 offset:45568\n"
"ds_read_b128 a[220:223], v134 offset:32768\n"
"s_xor_b32 s56, s86, 128\n"
"buffer_load_dwordx4 v226, s[20:23], s56 offen offset:2048 nt lds\n"
"v_mfma_f32_32x32x16_bf16 v[80:95], a[152:155], a[244:247], v[80:95]\n"
"ds_read_b64_tr_b16 a[180:181], v140 offset:45056\n"
"v_mfma_f32_32x32x16_bf16 v[96:111], a[156:159], a[228:231], v[96:111]\n"
"ds_read_b64_tr_b16 a[182:183], v140 offset:45568\n"
"v_mfma_f32_32x32x16_bf16 v[112:127], a[156:159], a[244:247], v[112:127]\n"
"ds_read_b64_tr_b16 a[184:185], v139 offset:46080\n"
"v_mfma_f32_32x32x16_bf16 v[0:15], a[160:163], a[232:235], v[0:15]\n"
"ds_read_b64_tr_b16 a[186:187], v139 offset:46592\n"
"s_waitcnt lgkmcnt(4)\n"
"v_accvgpr_read_b32 v248, a223\n"
"v_mfma_f32_32x32x16_bf16 v[16:31], a[160:163], a[248:251], v[16:31]\n"
"ds_read_b64_tr_b16 a[188:189], v140 offset:46080\n"
"s_add_u32 m0, s45, 16384\n"
"s_xor_b32 s56, s86, 16384\n"
"buffer_load_dwordx4 v224, s[16:19], s56 offen nt lds\n"
"v_mfma_f32_32x32x16_bf16 v[32:47], a[164:167], a[232:235], v[32:47]\n"
"ds_read_b64_tr_b16 a[190:191], v140 offset:46592\n"
"v_mfma_f32_32x32x16_bf16 v[48:63], a[164:167], a[248:251], v[48:63]\n"
"v_mfma_f32_32x32x16_bf16 v[64:79], a[168:171], a[232:235], v[64:79]\n"
"v_mfma_f32_32x32x16_bf16 v[80:95], a[168:171], a[248:251], v[80:95]\n"
"v_mfma_f32_32x32x16_bf16 v[96:111], a[172:175], a[232:235], v[96:111]\n"
"s_xor_b32 s56, s86, 16384\n"
"buffer_load_dwordx4 v225, s[16:19], s56 offen offset:2048 nt lds\n"
"v_mfma_f32_32x32x16_bf16 v[112:127], a[172:175], a[248:251], v[112:127]\n"
"v_mfma_f32_32x32x16_bf16 v[0:15], a[176:179], a[236:239], v[0:15]\n"
"s_waitcnt vmcnt(23)\n"
"s_waitcnt lgkmcnt(0)\n"
"s_barrier\n"
"ds_read_b64_tr_b16 a[128:129], v141 offset:0\n"
"ds_read_b64_tr_b16 a[130:131], v141 offset:512\n"
"v_mfma_f32_32x32x16_bf16 v[16:31], a[176:179], a[252:255], v[16:31]\n"
"ds_read_b64_tr_b16 a[132:133], v142 offset:0\n"
"ds_read_b64_tr_b16 a[134:135], v142 offset:512\n"
"v_mfma_f32_32x32x16_bf16 v[32:47], a[180:183], a[236:239], v[32:47]\n"
"ds_read_b64_tr_b16 a[136:137], v141 offset:1024\n"
"ds_read_b64_tr_b16 a[138:139], v141 offset:1536\n"
"v_mfma_f32_32x32x16_bf16 v[48:63], a[180:183], a[252:255], v[48:63]\n"
"ds_read_b64_tr_b16 a[140:141], v142 offset:1024\n"
"s_add_u32 m0, s45, 17408\n"
"s_xor_b32 s56, s86, 16512\n"
"buffer_load_dwordx4 v224, s[16:19], s56 offen nt lds\n"
"v_mfma_f32_32x32x16_bf16 v[64:79], a[184:187], a[236:239], v[64:79]\n"
"ds_read_b64_tr_b16 a[142:143], v142 offset:1536\n"
"ds_read_b64_tr_b16 a[144:145], v141 offset:4096\n"
"v_mfma_f32_32x32x16_bf16 v[80:95], a[184:187], a[252:255], v[80:95]\n"
"ds_read_b64_tr_b16 a[146:147], v141 offset:4608\n"
"ds_read_b64_tr_b16 a[148:149], v142 offset:4096\n"
"v_mfma_f32_32x32x16_bf16 v[96:111], a[188:191], a[236:239], v[96:111]\n"
"ds_read_b64_tr_b16 a[150:151], v142 offset:4608\n"
"ds_read_b64_tr_b16 a[152:153], v141 offset:5120\n"
"v_mfma_f32_32x32x16_bf16 v[112:127], a[188:191], a[252:255], v[112:127]\n"
"ds_read_b64_tr_b16 a[154:155], v141 offset:5632\n"
"s_waitcnt lgkmcnt(12)\n"
"v_mfma_f32_32x32x16_bf16 v[0:15], a[128:131], a[192:195], v[0:15]\n"
"ds_read_b64_tr_b16 a[156:157], v142 offset:5120\n"
"s_xor_b32 s56, s86, 16512\n"
"buffer_load_dwordx4 v225, s[16:19], s56 offen offset:2048 nt lds\n"
"v_mfma_f32_32x32x16_bf16 v[16:31], a[128:131], a[208:211], v[16:31]\n"
"ds_read_b64_tr_b16 a[158:159], v142 offset:5632\n"
"s_waitcnt lgkmcnt(10)\n"
"v_mfma_f32_32x32x16_bf16 v[32:47], a[132:135], a[192:195], v[32:47]\n"
"ds_read_b64_tr_b16 a[160:161], v141 offset:8192\n"
"s_waitcnt vmcnt(21)\n"
"ds_read_b128 a[224:227], v131 offset:49152\n"
"ds_read_b128 a[228:231], v132 offset:49152\n"
"v_mfma_f32_32x32x16_bf16 v[48:63], a[132:135], a[208:211], v[48:63]\n"
"ds_read_b64_tr_b16 a[162:163], v141 offset:8704\n"
"ds_read_b128 a[232:235], v133 offset:49152\n"
"v_mfma_f32_32x32x16_bf16 v[64:79], a[136:139], a[192:195], v[64:79]\n"
"ds_read_b64_tr_b16 a[164:165], v142 offset:8192\n"
"ds_read_b128 a[236:239], v134 offset:49152\n"
"v_mfma_f32_32x32x16_bf16 v[80:95], a[136:139], a[208:211], v[80:95]\n"
"ds_read_b64_tr_b16 a[166:167], v142 offset:8704\n"
"s_add_u32 m0, s45, 81920\n"
"s_xor_b32 s56, s86, 16384\n"
"buffer_load_dwordx4 v226, s[20:23], s56 offen nt lds\n"
"s_waitcnt lgkmcnt(10)\n"
"v_mfma_f32_32x32x16_bf16 v[96:111], a[140:143], a[192:195], v[96:111]\n"
"ds_read_b64_tr_b16 a[168:169], v141 offset:9216\n"
"v_mfma_f32_32x32x16_bf16 v[112:127], a[140:143], a[208:211], v[112:127]\n"
"ds_read_b64_tr_b16 a[170:171], v141 offset:9728\n"
"v_mfma_f32_32x32x16_bf16 v[0:15], a[144:147], a[196:199], v[0:15]\n"
"ds_read_b64_tr_b16 a[172:173], v142 offset:9216\n"
"s_waitcnt lgkmcnt(4)\n"
"v_accvgpr_read_b32 v248, a239\n"
"v_mfma_f32_32x32x16_bf16 v[16:31], a[144:147], a[212:215], v[16:31]\n"
"v_mfma_f32_32x32x16_bf16 v[32:47], a[148:151], a[196:199], v[32:47]\n"
"ds_read_b64_tr_b16 a[174:175], v142 offset:9728\n"
"s_waitcnt vmcnt(18)\n"
"ds_read_b128 a[240:243], v135 offset:49152\n"
"ds_read_b128 a[244:247], v136 offset:49152\n"
"s_xor_b32 s56, s86, 16384\n"
"buffer_load_dwordx4 v226, s[20:23], s56 offen offset:2048 nt lds\n"
"v_mfma_f32_32x32x16_bf16 v[48:63], a[148:151], a[212:215], v[48:63]\n"
"ds_read_b64_tr_b16 a[176:177], v141 offset:12288\n"
"ds_read_b128 a[248:251], v137 offset:49152\n"
"v_mfma_f32_32x32x16_bf16 v[64:79], a[152:155], a[196:199], v[64:79]\n"
"ds_read_b64_tr_b16 a[178:179], v141 offset:12800\n"
"ds_read_b128 a[252:255], v138 offset:49152\n"
"v_mfma_f32_32x32x16_bf16 v[80:95], a[152:155], a[212:215], v[80:95]\n"
"ds_read_b64_tr_b16 a[180:181], v142 offset:12288\n"
"v_mfma_f32_32x32x16_bf16 v[96:111], a[156:159], a[196:199], v[96:111]\n"
"ds_read_b64_tr_b16 a[182:183], v142 offset:12800\n"
"v_mfma_f32_32x32x16_bf16 v[112:127], a[156:159], a[212:215], v[112:127]\n"
"ds_read_b64_tr_b16 a[184:185], v141 offset:13312\n"
"s_add_u32 m0, s45, 82944\n"
"s_xor_b32 s56, s86, 16512\n"
"buffer_load_dwordx4 v226, s[20:23], s56 offen nt lds\n"
"v_mfma_f32_32x32x16_bf16 v[0:15], a[160:163], a[200:203], v[0:15]\n"
"ds_read_b64_tr_b16 a[186:187], v141 offset:13824\n"
"s_waitcnt lgkmcnt(4)\n"
"v_accvgpr_read_b32 v248, a255\n"
"v_mfma_f32_32x32x16_bf16 v[16:31], a[160:163], a[216:219], v[16:31]\n"
"ds_read_b64_tr_b16 a[188:189], v142 offset:13312\n"
"v_mfma_f32_32x32x16_bf16 v[32:47], a[164:167], a[200:203], v[32:47]\n"
"ds_read_b64_tr_b16 a[190:191], v142 offset:13824\n"
"v_mfma_f32_32x32x16_bf16 v[48:63], a[164:167], a[216:219], v[48:63]\n"
"v_mfma_f32_32x32x16_bf16 v[64:79], a[168:171], a[200:203], v[64:79]\n"
"s_xor_b32 s56, s86, 16512\n"
"buffer_load_dwordx4 v226, s[20:23], s56 offen offset:2048 nt lds\n"
"v_mfma_f32_32x32x16_bf16 v[80:95], a[168:171], a[216:219], v[80:95]\n"
"v_mfma_f32_32x32x16_bf16 v[96:111], a[172:175], a[200:203], v[96:111]\n"
"v_mfma_f32_32x32x16_bf16 v[112:127], a[172:175], a[216:219], v[112:127]\n"
"v_mfma_f32_32x32x16_bf16 v[0:15], a[176:179], a[204:207], v[0:15]\n"
"s_waitcnt vmcnt(17)\n"
"s_waitcnt lgkmcnt(0)\n"
"s_barrier\n"
"ds_read_b64_tr_b16 a[128:129], v141 offset:16384\n"
"ds_read_b64_tr_b16 a[130:131], v141 offset:16896\n"
"v_mfma_f32_32x32x16_bf16 v[16:31], a[176:179], a[220:223], v[16:31]\n"
"ds_read_b64_tr_b16 a[132:133], v141 offset:20480\n"
"ds_read_b64_tr_b16 a[134:135], v141 offset:20992\n"
"s_add_u32 m0, s45, 32768\n"
"s_xor_b32 s56, s86, 32768\n"
"buffer_load_dwordx4 v224, s[16:19], s56 offen nt lds\n"
"v_mfma_f32_32x32x16_bf16 v[32:47], a[180:183], a[204:207], v[32:47]\n"
"ds_read_b64_tr_b16 a[136:137], v141 offset:24576\n"
"ds_read_b64_tr_b16 a[138:139], v141 offset:25088\n"
"v_mfma_f32_32x32x16_bf16 v[48:63], a[180:183], a[220:223], v[48:63]\n"
"ds_read_b64_tr_b16 a[140:141], v141 offset:28672\n"
"v_mfma_f32_32x32x16_bf16 v[64:79], a[184:187], a[204:207], v[64:79]\n"
"ds_read_b64_tr_b16 a[142:143], v141 offset:29184\n"
"ds_read_b64_tr_b16 a[144:145], v142 offset:16384\n"
"v_mfma_f32_32x32x16_bf16 v[80:95], a[184:187], a[220:223], v[80:95]\n"
"ds_read_b64_tr_b16 a[146:147], v142 offset:16896\n"
"ds_read_b64_tr_b16 a[148:149], v142 offset:20480\n"
"v_mfma_f32_32x32x16_bf16 v[96:111], a[188:191], a[204:207], v[96:111]\n"
"ds_read_b64_tr_b16 a[150:151], v142 offset:20992\n"
"ds_read_b64_tr_b16 a[152:153], v142 offset:24576\n"
"s_xor_b32 s56, s86, 32768\n"
"buffer_load_dwordx4 v225, s[16:19], s56 offen offset:2048 nt lds\n"
"v_mfma_f32_32x32x16_bf16 v[112:127], a[188:191], a[220:223], v[112:127]\n"
"ds_read_b64_tr_b16 a[154:155], v142 offset:25088\n"
"s_waitcnt lgkmcnt(12)\n"
"v_mfma_f32_32x32x16_bf16 v[0:15], a[128:131], a[224:227], v[0:15]\n"
"s_mov_b32 s57, 0x3e0293ee\n"
"ds_read_b64_tr_b16 a[156:157], v142 offset:28672\n"
"ds_read_b64_tr_b16 a[158:159], v142 offset:29184\n"
"s_waitcnt lgkmcnt(12)\n"
"v_mfma_f32_32x32x16_bf16 v[0:15], a[132:135], a[228:231], v[0:15]\n"
"ds_read_b64_tr_b16 a[160:161], v141 offset:17408\n"
"s_waitcnt lgkmcnt(11)\n"
"v_mfma_f32_32x32x16_bf16 v[0:15], a[136:139], a[232:235], v[0:15]\n"
"ds_read_b64_tr_b16 a[162:163], v141 offset:17920\n"
"s_waitcnt lgkmcnt(10)\n"
"v_mfma_f32_32x32x16_bf16 v[0:15], a[140:143], a[236:239], v[0:15]\n"
"ds_read_b64_tr_b16 a[164:165], v141 offset:21504\n"
"s_add_u32 m0, s45, 33792\n"
"s_xor_b32 s56, s86, 32896\n"
"buffer_load_dwordx4 v224, s[16:19], s56 offen nt lds\n"
"v_mfma_f32_32x32x16_bf16 v[16:31], a[128:131], a[240:243], v[16:31]\n"
"ds_read_b64_tr_b16 a[166:167], v141 offset:22016\n"
"v_mfma_f32_32x32x16_bf16 v[16:31], a[132:135], a[244:247], v[16:31]\n"
"ds_read_b64_tr_b16 a[168:169], v141 offset:25600\n"
"v_mfma_f32_32x32x16_bf16 v[16:31], a[136:139], a[248:251], v[16:31]\n"
"ds_read_b64_tr_b16 a[170:171], v141 offset:26112\n"
"v_mfma_f32_32x32x16_bf16 v[16:31], a[140:143], a[252:255], v[16:31]\n"
"ds_read_b64_tr_b16 a[172:173], v141 offset:29696\n"
"s_waitcnt lgkmcnt(9)\n"
"v_mfma_f32_32x32x16_bf16 v[32:47], a[144:147], a[224:227], v[32:47]\n"
"ds_read_b64_tr_b16 a[174:175], v141 offset:30208\n"
"s_xor_b32 s56, s86, 32896\n"
"buffer_load_dwordx4 v225, s[16:19], s56 offen offset:2048 nt lds\n"
"v_mfma_f32_32x32x16_bf16 v[32:47], a[148:151], a[228:231], v[32:47]\n"
"ds_read_b64_tr_b16 a[176:177], v142 offset:17408\n"
"v_mfma_f32_32x32x16_bf16 v[32:47], a[152:155], a[232:235], v[32:47]\n"
"ds_read_b64_tr_b16 a[178:179], v142 offset:17920\n"
"v_mul_f32 v0, s57, v0\n"
"v_mul_f32 v1, s57, v1\n"
"v_mul_f32 v2, s57, v2\n"
"v_mul_f32 v3, s57, v3\n"
"v_mul_f32 v4, s57, v4\n"
"v_mul_f32 v5, s57, v5\n"
"s_waitcnt vmcnt(19)\n"
"v_add_u32 v247, 1, v247\n"
"s_mov_b64 exec, s[46:47]\n"
"buffer_store_dword v247, v246, s[40:43], 0 offen sc1\n"
"s_mov_b64 exec, -1\n"
"s_waitcnt lgkmcnt(5)\n"
"v_mfma_f32_32x32x16_bf16 v[32:47], a[156:159], a[236:239], v[32:47]\n"
"ds_read_b64_tr_b16 a[180:181], v142 offset:21504\n"
"v_mul_f32 v6, s57, v6\n"
"v_mul_f32 v7, s57, v7\n"
"v_mul_f32 v8, s57, v8\n"
"v_mul_f32 v9, s57, v9\n"
"v_mul_f32 v10, s57, v10\n"
"v_mul_f32 v11, s57, v11\n"
"v_mfma_f32_32x32x16_bf16 v[48:63], a[144:147], a[240:243], v[48:63]\n"
"ds_read_b64_tr_b16 a[182:183], v142 offset:22016\n"
"v_mul_f32 v12, s57, v12\n"
"v_mul_f32 v13, s57, v13\n"
"v_mul_f32 v14, s57, v14\n"
"v_mul_f32 v15, s57, v15\n"
"v_cvt_pk_bf16_f32 v160, v0, v1\n"
"v_cvt_pk_bf16_f32 v161, v2, v3\n"
"v_mfma_f32_32x32x16_bf16 v[48:63], a[148:151], a[244:247], v[48:63]\n"
"ds_read_b64_tr_b16 a[184:185], v142 offset:25600\n"
"v_cvt_pk_bf16_f32 v162, v4, v5\n"
"v_cvt_pk_bf16_f32 v163, v6, v7\n"
"v_cvt_pk_bf16_f32 v164, v8, v9\n"
"v_cvt_pk_bf16_f32 v165, v10, v11\n"
"v_cvt_pk_bf16_f32 v166, v12, v13\n"
"v_cvt_pk_bf16_f32 v167, v14, v15\n"
"s_add_u32 m0, s45, 98304\n"
"s_xor_b32 s56, s86, 32768\n"
"buffer_load_dwordx4 v226, s[20:23], s56 offen nt lds\n"
"v_mfma_f32_32x32x16_bf16 v[48:63], a[152:155], a[248:251], v[48:63]\n"
"ds_read_b64_tr_b16 a[186:187], v142 offset:26112\n"
"v_mul_f32 v16, s57, v16\n"
"v_mul_f32 v17, s57, v17\n"
"v_mul_f32 v18, s57, v18\n"
"v_mul_f32 v19, s57, v19\n"
"v_mul_f32 v20, s57, v20\n"
"v_mul_f32 v21, s57, v21\n"
"v_mfma_f32_32x32x16_bf16 v[48:63], a[156:159], a[252:255], v[48:63]\n"
"ds_read_b64_tr_b16 a[188:189], v142 offset:29696\n"
"v_mul_f32 v22, s57, v22\n"
"v_mul_f32 v23, s57, v23\n"
"v_mul_f32 v24, s57, v24\n"
"v_mul_f32 v25, s57, v25\n"
"v_mul_f32 v26, s57, v26\n"
"v_mul_f32 v27, s57, v27\n"
"v_mfma_f32_32x32x16_bf16 v[64:79], a[160:163], a[224:227], v[64:79]\n"
"ds_read_b64_tr_b16 a[190:191], v142 offset:30208\n"
"v_mul_f32 v28, s57, v28\n"
"v_mul_f32 v29, s57, v29\n"
"v_mul_f32 v30, s57, v30\n"
"v_mul_f32 v31, s57, v31\n"
"v_cvt_pk_bf16_f32 v192, v16, v17\n"
"v_cvt_pk_bf16_f32 v193, v18, v19\n"
"v_mfma_f32_32x32x16_bf16 v[64:79], a[164:167], a[228:231], v[64:79]\n"
"v_cvt_pk_bf16_f32 v194, v20, v21\n"
"v_cvt_pk_bf16_f32 v195, v22, v23\n"
"v_cvt_pk_bf16_f32 v196, v24, v25\n"
"v_cvt_pk_bf16_f32 v197, v26, v27\n"
"v_cvt_pk_bf16_f32 v198, v28, v29\n"
"v_cvt_pk_bf16_f32 v199, v30, v31\n"
"s_waitcnt lgkmcnt(2)\n"
"v_mfma_f32_32x32x16_bf16 v[64:79], a[168:171], a[232:235], v[64:79]\n"
"v_mul_f32 v32, s57, v32\n"
"v_mul_f32 v33, s57, v33\n"
"v_mul_f32 v34, s57, v34\n"
"v_mul_f32 v35, s57, v35\n"
"v_mul_f32 v36, s57, v36\n"
"v_mul_f32 v37, s57, v37\n"
"s_xor_b32 s56, s86, 32768\n"
"buffer_load_dwordx4 v226, s[20:23], s56 offen offset:2048 nt lds\n"
"v_mfma_f32_32x32x16_bf16 v[64:79], a[172:175], a[236:239], v[64:79]\n"
"v_mul_f32 v38, s57, v38\n"
"v_mul_f32 v39, s57, v39\n"
"v_mul_f32 v40, s57, v40\n"
"v_mul_f32 v41, s57, v41\n"
"v_mul_f32 v42, s57, v42\n"
"v_mul_f32 v43, s57, v43\n"
"v_mfma_f32_32x32x16_bf16 v[80:95], a[160:163], a[240:243], v[80:95]\n"
"v_mul_f32 v44, s57, v44\n"
"v_mul_f32 v45, s57, v45\n"
"v_mul_f32 v46, s57, v46\n"
"v_mul_f32 v47, s57, v47\n"
"v_cvt_pk_bf16_f32 v168, v32, v33\n"
"v_cvt_pk_bf16_f32 v169, v34, v35\n"
"v_mfma_f32_32x32x16_bf16 v[80:95], a[164:167], a[244:247], v[80:95]\n"
"v_cvt_pk_bf16_f32 v170, v36, v37\n"
"v_cvt_pk_bf16_f32 v171, v38, v39\n"
"v_cvt_pk_bf16_f32 v172, v40, v41\n"
"v_cvt_pk_bf16_f32 v173, v42, v43\n"
"v_cvt_pk_bf16_f32 v174, v44, v45\n"
"v_cvt_pk_bf16_f32 v175, v46, v47\n"
"v_mfma_f32_32x32x16_bf16 v[80:95], a[168:171], a[248:251], v[80:95]\n"
"v_mul_f32 v48, s57, v48\n"
"v_mul_f32 v49, s57, v49\n"
"v_mul_f32 v50, s57, v50\n"
"v_mul_f32 v51, s57, v51\n"
"v_mul_f32 v52, s57, v52\n"
"v_mul_f32 v53, s57, v53\n"
"v_mfma_f32_32x32x16_bf16 v[80:95], a[172:175], a[252:255], v[80:95]\n"
"v_mul_f32 v54, s57, v54\n"
"v_mul_f32 v55, s57, v55\n"
"v_mul_f32 v56, s57, v56\n"
"v_mul_f32 v57, s57, v57\n"
"v_mul_f32 v58, s57, v58\n"
"v_mul_f32 v59, s57, v59\n"
"s_add_u32 m0, s45, 99328\n"
"s_xor_b32 s56, s86, 32896\n"
"buffer_load_dwordx4 v226, s[20:23], s56 offen nt lds\n"
"v_mfma_f32_32x32x16_bf16 v[96:111], a[176:179], a[224:227], v[96:111]\n"
"v_mul_f32 v60, s57, v60\n"
"v_mul_f32 v61, s57, v61\n"
"v_mul_f32 v62, s57, v62\n"
"v_mul_f32 v63, s57, v63\n"
"v_cvt_pk_bf16_f32 v200, v48, v49\n"
"v_cvt_pk_bf16_f32 v201, v50, v51\n"
"s_waitcnt vmcnt(20)\n"
"s_waitcnt lgkmcnt(0)\n"
"s_barrier\n"
"ds_read_b128 a[128:131], v229 offset:0\n"
"v_mfma_f32_32x32x16_bf16 v[96:111], a[180:183], a[228:231], v[96:111]\n"
"v_cvt_pk_bf16_f32 v202, v52, v53\n"
"v_cvt_pk_bf16_f32 v203, v54, v55\n"
"v_cvt_pk_bf16_f32 v204, v56, v57\n"
"v_cvt_pk_bf16_f32 v205, v58, v59\n"
"v_cvt_pk_bf16_f32 v206, v60, v61\n"
"v_cvt_pk_bf16_f32 v207, v62, v63\n"
"ds_read_b128 a[132:135], v230 offset:0\n"
"v_mfma_f32_32x32x16_bf16 v[96:111], a[184:187], a[232:235], v[96:111]\n"
"v_mul_f32 v64, s57, v64\n"
"v_mul_f32 v65, s57, v65\n"
"v_mul_f32 v66, s57, v66\n"
"v_mul_f32 v67, s57, v67\n"
"v_mul_f32 v68, s57, v68\n"
"v_mul_f32 v69, s57, v69\n"
"v_mul_f32 v70, s57, v70\n"
"v_mul_f32 v71, s57, v71\n"
"ds_read_b128 a[136:139], v231 offset:0\n"
"v_mfma_f32_32x32x16_bf16 v[96:111], a[188:191], a[236:239], v[96:111]\n"
"v_mul_f32 v72, s57, v72\n"
"v_mul_f32 v73, s57, v73\n"
"v_mul_f32 v74, s57, v74\n"
"v_mul_f32 v75, s57, v75\n"
"v_mul_f32 v76, s57, v76\n"
"v_mul_f32 v77, s57, v77\n"
"v_mul_f32 v78, s57, v78\n"
"v_mul_f32 v79, s57, v79\n"
"ds_read_b128 a[140:143], v232 offset:0\n"
"v_mfma_f32_32x32x16_bf16 v[112:127], a[176:179], a[240:243], v[112:127]\n"
"v_cvt_pk_bf16_f32 v176, v64, v65\n"
"v_cvt_pk_bf16_f32 v177, v66, v67\n"
"v_cvt_pk_bf16_f32 v178, v68, v69\n"
"v_cvt_pk_bf16_f32 v179, v70, v71\n"
"v_cvt_pk_bf16_f32 v180, v72, v73\n"
"v_cvt_pk_bf16_f32 v181, v74, v75\n"
"v_cvt_pk_bf16_f32 v182, v76, v77\n"
"v_cvt_pk_bf16_f32 v183, v78, v79\n"
"ds_read_b128 a[144:147], v229 offset:1024\n"
"s_xor_b32 s56, s86, 32896\n"
"buffer_load_dwordx4 v226, s[20:23], s56 offen offset:2048 nt lds\n"
"v_mfma_f32_32x32x16_bf16 v[112:127], a[180:183], a[244:247], v[112:127]\n"
"v_mul_f32 v80, s57, v80\n"
"v_mul_f32 v81, s57, v81\n"
"v_mul_f32 v82, s57, v82\n"
"v_mul_f32 v83, s57, v83\n"
"v_mul_f32 v84, s57, v84\n"
"v_mul_f32 v85, s57, v85\n"
"v_mul_f32 v86, s57, v86\n"
"v_mul_f32 v87, s57, v87\n"
"ds_read_b128 a[148:151], v230 offset:1024\n"
"v_mfma_f32_32x32x16_bf16 v[112:127], a[184:187], a[248:251], v[112:127]\n"
"v_mul_f32 v88, s57, v88\n"
"v_mul_f32 v89, s57, v89\n"
"v_mul_f32 v90, s57, v90\n"
"v_mul_f32 v91, s57, v91\n"
"v_mul_f32 v92, s57, v92\n"
"v_mul_f32 v93, s57, v93\n"
"v_mul_f32 v94, s57, v94\n"
"v_mul_f32 v95, s57, v95\n"
"ds_read_b128 a[152:155], v231 offset:1024\n"
"v_mfma_f32_32x32x16_bf16 v[112:127], a[188:191], a[252:255], v[112:127]\n"
"v_cvt_pk_bf16_f32 v208, v80, v81\n"
"v_cvt_pk_bf16_f32 v209, v82, v83\n"
"v_cvt_pk_bf16_f32 v210, v84, v85\n"
"v_cvt_pk_bf16_f32 v211, v86, v87\n"
"v_cvt_pk_bf16_f32 v212, v88, v89\n"
"v_cvt_pk_bf16_f32 v213, v90, v91\n"
"v_cvt_pk_bf16_f32 v214, v92, v93\n"
"v_cvt_pk_bf16_f32 v215, v94, v95\n"
"s_waitcnt lgkmcnt(2)\n"
"v_mfma_f32_32x32x16_bf16 v[0:15], a[128:131], v[160:163], 0\n"
"ds_read_b128 a[156:159], v232 offset:1024\n"
"v_mfma_f32_32x32x16_bf16 v[16:31], a[128:131], v[192:195], 0\n"
"s_add_u32 m0, s45, 49152\n"
"s_xor_b32 s56, s86, 49152\n"
"buffer_load_dwordx4 v224, s[16:19], s56 offen nt lds\n"
"v_mfma_f32_32x32x16_bf16 v[0:15], a[132:135], v[164:167], v[0:15]\n"
"v_mul_f32 v96, s57, v96\n"
"v_mul_f32 v97, s57, v97\n"
"v_mul_f32 v98, s57, v98\n"
"v_mul_f32 v99, s57, v99\n"
"v_mul_f32 v100, s57, v100\n"
"v_mul_f32 v101, s57, v101\n"
"ds_read_b128 a[160:163], v229 offset:8192\n"
"v_mfma_f32_32x32x16_bf16 v[16:31], a[132:135], v[196:199], v[16:31]\n"
"v_mul_f32 v102, s57, v102\n"
"v_mul_f32 v103, s57, v103\n"
"v_mul_f32 v104, s57, v104\n"
"v_mul_f32 v105, s57, v105\n"
"v_mul_f32 v106, s57, v106\n"
"v_mul_f32 v107, s57, v107\n"
"v_mfma_f32_32x32x16_bf16 v[0:15], a[136:139], v[168:171], v[0:15]\n"
"v_mul_f32 v108, s57, v108\n"
"v_mul_f32 v109, s57, v109\n"
"v_mul_f32 v110, s57, v110\n"
"v_mul_f32 v111, s57, v111\n"
"v_cvt_pk_bf16_f32 v184, v96, v97\n"
"v_cvt_pk_bf16_f32 v185, v98, v99\n"
"ds_read_b128 a[164:167], v230 offset:8192\n"
"v_mfma_f32_32x32x16_bf16 v[16:31], a[136:139], v[200:203], v[16:31]\n"
"v_cvt_pk_bf16_f32 v186, v100, v101\n"
"v_cvt_pk_bf16_f32 v187, v102, v103\n"
"v_cvt_pk_bf16_f32 v188, v104, v105\n"
"v_cvt_pk_bf16_f32 v189, v106, v107\n"
"v_cvt_pk_bf16_f32 v190, v108, v109\n"
"v_cvt_pk_bf16_f32 v191, v110, v111\n"
"v_mfma_f32_32x32x16_bf16 v[0:15], a[140:143], v[172:175], v[0:15]\n"
"v_mul_f32 v112, s57, v112\n"
"v_mul_f32 v113, s57, v113\n"
"v_mul_f32 v114, s57, v114\n"
"v_mul_f32 v115, s57, v115\n"
"v_mul_f32 v116, s57, v116\n"
"v_mul_f32 v117, s57, v117\n"
"ds_read_b128 a[168:171], v231 offset:8192\n"
"s_xor_b32 s56, s86, 49152\n"
"buffer_load_dwordx4 v225, s[16:19], s56 offen offset:2048 nt lds\n"
"v_mfma_f32_32x32x16_bf16 v[16:31], a[140:143], v[204:207], v[16:31]\n"
"v_mul_f32 v118, s57, v118\n"
"v_mul_f32 v119, s57, v119\n"
"v_mul_f32 v120, s57, v120\n"
"v_mul_f32 v121, s57, v121\n"
"v_mul_f32 v122, s57, v122\n"
"v_mul_f32 v123, s57, v123\n"
"v_mfma_f32_32x32x16_bf16 v[0:15], a[144:147], v[176:179], v[0:15]\n"
"v_mul_f32 v124, s57, v124\n"
"v_mul_f32 v125, s57, v125\n"
"v_mul_f32 v126, s57, v126\n"
"v_mul_f32 v127, s57, v127\n"
"v_cvt_pk_bf16_f32 v216, v112, v113\n"
"v_cvt_pk_bf16_f32 v217, v114, v115\n"
"ds_read_b128 a[172:175], v232 offset:8192\n"
"v_mfma_f32_32x32x16_bf16 v[16:31], a[144:147], v[208:211], v[16:31]\n"
"v_cvt_pk_bf16_f32 v218, v116, v117\n"
"v_cvt_pk_bf16_f32 v219, v118, v119\n"
"v_cvt_pk_bf16_f32 v220, v120, v121\n"
"v_cvt_pk_bf16_f32 v221, v122, v123\n"
"v_cvt_pk_bf16_f32 v222, v124, v125\n"
"v_cvt_pk_bf16_f32 v223, v126, v127\n"
"s_waitcnt lgkmcnt(1)\n"
"v_mfma_f32_32x32x16_bf16 v[0:15], a[148:151], v[180:183], v[0:15]\n"
"s_mul_i32 s48, s87, 8\n"
"ds_read_b128 a[176:179], v229 offset:9216\n"
"v_mfma_f32_32x32x16_bf16 v[16:31], a[148:151], v[212:215], v[16:31]\n"
"s_add_u32 m0, s45, 50176\n"
"s_xor_b32 s56, s86, 49280\n"
"buffer_load_dwordx4 v224, s[16:19], s56 offen nt lds\n"
"v_mfma_f32_32x32x16_bf16 v[0:15], a[152:155], v[184:187], v[0:15]\n"
"s_lshl_b32 s49, s48, 14\n"
"s_lshl_b32 s48, s48, 11\n"
"ds_read_b128 a[180:183], v230 offset:9216\n"
"v_mfma_f32_32x32x16_bf16 v[16:31], a[152:155], v[216:219], v[16:31]\n"
"v_mfma_f32_32x32x16_bf16 v[0:15], a[156:159], v[188:191], v[0:15]\n"
"ds_read_b128 a[184:187], v231 offset:9216\n"
"v_mfma_f32_32x32x16_bf16 v[16:31], a[156:159], v[220:223], v[16:31]\n"
"v_mfma_f32_32x32x16_bf16 v[32:47], a[160:163], v[160:163], 0\n"
"ds_read_b128 a[188:191], v232 offset:9216\n"
"s_xor_b32 s56, s86, 49280\n"
"buffer_load_dwordx4 v225, s[16:19], s56 offen offset:2048 nt lds\n"
"v_mfma_f32_32x32x16_bf16 v[48:63], a[160:163], v[192:195], 0\n"
"v_mfma_f32_32x32x16_bf16 v[32:47], a[164:167], v[164:167], v[32:47]\n"
"v_mfma_f32_32x32x16_bf16 v[48:63], a[164:167], v[196:199], v[48:63]\n"
"s_nop 2\n"
"v_max_f32 v239, v0, v1\n"
"s_nop 0\n"
"v_max_f32 v240, v16, v17\n"
"v_mfma_f32_32x32x16_bf16 v[32:47], a[168:171], v[168:171], v[32:47]\n"
"v_max3_f32 v239, v239, v2, v3\n"
"v_mfma_f32_32x32x16_bf16 v[48:63], a[168:171], v[200:203], v[48:63]\n"
"v_max3_f32 v240, v240, v18, v19\n"
"s_add_u32 m0, s45, 114688\n"
"s_xor_b32 s56, s86, 49152\n"
"buffer_load_dwordx4 v226, s[20:23], s56 offen nt lds\n"
"s_waitcnt lgkmcnt(2)\n"
"v_mfma_f32_32x32x16_bf16 v[32:47], a[172:175], v[172:175], v[32:47]\n"
"v_max3_f32 v239, v239, v4, v5\n"
"v_mfma_f32_32x32x16_bf16 v[48:63], a[172:175], v[204:207], v[48:63]\n"
"v_max3_f32 v240, v240, v20, v21\n"
"v_max3_f32 v239, v239, v6, v7\n"
"v_mfma_f32_32x32x16_bf16 v[32:47], a[176:179], v[176:179], v[32:47]\n"
"v_max3_f32 v240, v240, v22, v23\n"
"v_mfma_f32_32x32x16_bf16 v[48:63], a[176:179], v[208:211], v[48:63]\n"
"v_max3_f32 v239, v239, v8, v9\n"
"v_mfma_f32_32x32x16_bf16 v[32:47], a[180:183], v[180:183], v[32:47]\n"
"v_max3_f32 v240, v240, v24, v25\n"
"s_xor_b32 s56, s86, 49152\n"
"buffer_load_dwordx4 v226, s[20:23], s56 offen offset:2048 nt lds\n"
"v_mfma_f32_32x32x16_bf16 v[48:63], a[180:183], v[212:215], v[48:63]\n"
"v_max3_f32 v239, v239, v10, v11\n"
"v_max3_f32 v240, v240, v26, v27\n"
"s_waitcnt lgkmcnt(0)\n"
"v_mfma_f32_32x32x16_bf16 v[32:47], a[184:187], v[184:187], v[32:47]\n"
"v_max3_f32 v239, v239, v12, v13\n"
"v_mfma_f32_32x32x16_bf16 v[48:63], a[184:187], v[216:219], v[48:63]\n"
"v_max3_f32 v240, v240, v28, v29\n"
"v_mfma_f32_32x32x16_bf16 v[32:47], a[188:191], v[188:191], v[32:47]\n"
"v_max3_f32 v239, v239, v14, v15\n"
"v_mfma_f32_32x32x16_bf16 v[48:63], a[188:191], v[220:223], v[48:63]\n"
"v_max3_f32 v240, v240, v30, v31\n"
"s_add_u32 m0, s45, 115712\n"
"s_xor_b32 s56, s86, 49280\n"
"buffer_load_dwordx4 v226, s[20:23], s56 offen nt lds\n"
"s_waitcnt vmcnt(20)\n"
"s_barrier\n"
"ds_read_b128 a[128:131], v229 offset:16384\n"
"ds_read_b128 a[132:135], v230 offset:16384\n"
"ds_read_b128 a[136:139], v231 offset:16384\n"
"ds_read_b128 a[140:143], v232 offset:16384\n"
"ds_read_b128 a[144:147], v229 offset:17408\n"
"ds_read_b128 a[148:151], v230 offset:17408\n"
"ds_read_b128 a[152:155], v231 offset:17408\n"
"v_max3_f32 v239, v239, v32, v33\n"
"v_max3_f32 v240, v240, v48, v49\n"
"v_max3_f32 v239, v239, v34, v35\n"
"v_max3_f32 v240, v240, v50, v51\n"
"v_max3_f32 v239, v239, v36, v37\n"
"v_max3_f32 v240, v240, v52, v53\n"
"v_max3_f32 v239, v239, v38, v39\n"
"v_max3_f32 v240, v240, v54, v55\n"
"v_max3_f32 v239, v239, v40, v41\n"
"v_max3_f32 v240, v240, v56, v57\n"
"v_max3_f32 v239, v239, v42, v43\n"
"v_max3_f32 v240, v240, v58, v59\n"
"v_max3_f32 v239, v239, v44, v45\n"
"v_max3_f32 v240, v240, v60, v61\n"
"v_max3_f32 v239, v239, v46, v47\n"
"v_max3_f32 v240, v240, v62, v63\n"
"v_mov_b32 v241, v239\n"
"s_nop 1\n"
"v_permlane32_swap_b32 v239, v241\n"
"v_max_f32 v239, v239, v241\n"
"v_mov_b32 v242, v240\n"
"s_nop 1\n"
"v_permlane32_swap_b32 v240, v242\n"
"v_max_f32 v240, v240, v242\n"
"v_sub_f32 v128, 0, v239\n"
"v_sub_f32 v129, 0, v239\n"
"v_sub_f32 v130, 0, v239\n"
"v_sub_f32 v131, 0, v239\n"
"v_sub_f32 v132, 0, v239\n"
"v_sub_f32 v133, 0, v239\n"
"v_sub_f32 v134, 0, v239\n"
"v_sub_f32 v135, 0, v239\n"
"v_sub_f32 v136, 0, v239\n"
"v_sub_f32 v137, 0, v239\n"
"v_sub_f32 v138, 0, v239\n"
"v_sub_f32 v139, 0, v239\n"
"v_sub_f32 v140, 0, v239\n"
"v_sub_f32 v141, 0, v239\n"
"v_sub_f32 v142, 0, v239\n"
"v_sub_f32 v143, 0, v239\n"
"v_sub_f32 v144, 0, v240\n"
"v_sub_f32 v145, 0, v240\n"
"v_sub_f32 v146, 0, v240\n"
"v_sub_f32 v147, 0, v240\n"
"v_sub_f32 v148, 0, v240\n"
"v_sub_f32 v149, 0, v240\n"
"v_sub_f32 v150, 0, v240\n"
"v_sub_f32 v151, 0, v240\n"
"v_sub_f32 v152, 0, v240\n"
"v_sub_f32 v153, 0, v240\n"
"v_sub_f32 v154, 0, v240\n"
"v_sub_f32 v155, 0, v240\n"
"v_sub_f32 v156, 0, v240\n"
"v_sub_f32 v157, 0, v240\n"
"v_sub_f32 v158, 0, v240\n"
"v_sub_f32 v159, 0, v240\n"
"v_add_f32 v0, v128, v0\n"
"v_add_f32 v1, v128, v1\n"
"v_exp_f32 v0, v0\n"
"v_exp_f32 v1, v1\n"
"v_add_f32 v2, v128, v2\n"
"v_add_f32 v3, v128, v3\n"
"v_exp_f32 v2, v2\n"
"v_mov_b32 v235, v0\n"
"v_exp_f32 v3, v3\n"
"v_mov_b32 v236, v1\n"
"v_cvt_pk_bf16_f32 v0, v0, v1\n"
"v_add_f32 v4, v128, v4\n"
"v_add_f32 v5, v128, v5\n"
"v_exp_f32 v4, v4\n"
"v_add_f32 v235, v235, v2\n"
"v_exp_f32 v5, v5\n"
"v_add_f32 v236, v236, v3\n"
"v_cvt_pk_bf16_f32 v1, v2, v3\n"
"v_add_f32 v6, v128, v6\n"
"v_add_f32 v7, v128, v7\n"
"v_exp_f32 v6, v6\n"
"v_add_f32 v235, v235, v4\n"
"v_exp_f32 v7, v7\n"
"v_add_f32 v236, v236, v5\n"
"v_cvt_pk_bf16_f32 v2, v4, v5\n"
"v_add_f32 v16, v144, v16\n"
"v_add_f32 v17, v144, v17\n"
"v_exp_f32 v16, v16\n"
"s_waitcnt lgkmcnt(0)\n"
"v_mfma_f32_32x32x16_bf16 v[64:79], a[128:131], v[160:163], v[128:143]\n"
"ds_read_b128 a[156:159], v232 offset:17408\n"
"v_add_f32 v235, v235, v6\n"
"v_exp_f32 v17, v17\n"
"v_add_f32 v236, v236, v7\n"
"v_cvt_pk_bf16_f32 v3, v6, v7\n"
"v_add_f32 v18, v144, v18\n"
"v_add_f32 v19, v144, v19\n"
"v_mfma_f32_32x32x16_bf16 v[80:95], a[128:131], v[192:195], v[144:159]\n"
"v_exp_f32 v18, v18\n"
"v_mov_b32 v237, v16\n"
"v_exp_f32 v19, v19\n"
"v_mov_b32 v238, v17\n"
"v_cvt_pk_bf16_f32 v16, v16, v17\n"
"v_add_f32 v20, v144, v20\n"
"v_mfma_f32_32x32x16_bf16 v[64:79], a[132:135], v[164:167], v[64:79]\n"
"ds_read_b128 a[160:163], v229 offset:24576\n"
"v_add_f32 v21, v144, v21\n"
"v_exp_f32 v20, v20\n"
"v_add_f32 v237, v237, v18\n"
"v_exp_f32 v21, v21\n"
"v_add_f32 v238, v238, v19\n"
"v_mfma_f32_32x32x16_bf16 v[80:95], a[132:135], v[196:199], v[80:95]\n"
"v_cvt_pk_bf16_f32 v17, v18, v19\n"
"v_add_f32 v22, v144, v22\n"
"v_add_f32 v23, v144, v23\n"
"v_exp_f32 v22, v22\n"
"v_add_f32 v237, v237, v20\n"
"v_exp_f32 v23, v23\n"
"v_mfma_f32_32x32x16_bf16 v[64:79], a[136:139], v[168:171], v[64:79]\n"
"ds_read_b128 a[164:167], v230 offset:24576\n"
"s_xor_b32 s56, s86, 49280\n"
"buffer_load_dwordx4 v226, s[20:23], s56 offen offset:2048 nt lds\n"
"v_add_f32 v238, v238, v21\n"
"v_mfma_f32_32x32x16_bf16 v[80:95], a[136:139], v[200:203], v[80:95]\n"
"v_cvt_pk_bf16_f32 v18, v20, v21\n"
"v_add_f32 v8, v128, v8\n"
"v_add_f32 v9, v128, v9\n"
"v_exp_f32 v8, v8\n"
"v_add_f32 v237, v237, v22\n"
"v_exp_f32 v9, v9\n"
"v_mfma_f32_32x32x16_bf16 v[64:79], a[140:143], v[172:175], v[64:79]\n"
"ds_read_b128 a[168:171], v231 offset:24576\n"
"v_add_f32 v238, v238, v23\n"
"v_cvt_pk_bf16_f32 v19, v22, v23\n"
"v_add_f32 v10, v128, v10\n"
"v_add_f32 v11, v128, v11\n"
"v_exp_f32 v10, v10\n"
"v_add_f32 v235, v235, v8\n"
"v_mfma_f32_32x32x16_bf16 v[80:95], a[140:143], v[204:207], v[80:95]\n"
"v_exp_f32 v11, v11\n"
"v_add_f32 v236, v236, v9\n"
"v_cvt_pk_bf16_f32 v8, v8, v9\n"
"v_add_f32 v12, v128, v12\n"
"v_add_f32 v13, v128, v13\n"
"v_exp_f32 v12, v12\n"
"v_mfma_f32_32x32x16_bf16 v[64:79], a[144:147], v[176:179], v[64:79]\n"
"ds_read_b128 a[172:175], v232 offset:24576\n"
"v_add_f32 v235, v235, v10\n"
"v_exp_f32 v13, v13\n"
"v_add_f32 v236, v236, v11\n"
"v_cvt_pk_bf16_f32 v9, v10, v11\n"
"v_add_f32 v14, v128, v14\n"
"v_add_f32 v15, v128, v15\n"
"v_mfma_f32_32x32x16_bf16 v[80:95], a[144:147], v[208:211], v[80:95]\n"
"s_add_u32 m0, s45, 131072\n"
"s_xor_b32 s56, s86, 65536\n"
"buffer_load_dwordx4 v226, s[20:23], s56 offen nt lds\n"
"v_exp_f32 v14, v14\n"
"v_mfma_f32_32x32x16_bf16 v[64:79], a[148:151], v[180:183], v[64:79]\n"
"ds_read_b128 a[176:179], v229 offset:25600\n"
"v_add_f32 v235, v235, v12\n"
"v_exp_f32 v15, v15\n"
"v_add_f32 v236, v236, v13\n"
"v_cvt_pk_bf16_f32 v10, v12, v13\n"
"v_add_f32 v24, v144, v24\n"
"v_add_f32 v25, v144, v25\n"
"v_mfma_f32_32x32x16_bf16 v[80:95], a[148:151], v[212:215], v[80:95]\n"
"v_exp_f32 v24, v24\n"
"v_add_f32 v235, v235, v14\n"
"v_exp_f32 v25, v25\n"
"v_add_f32 v236, v236, v15\n"
"v_cvt_pk_bf16_f32 v11, v14, v15\n"
"v_add_f32 v26, v144, v26\n"
"v_mfma_f32_32x32x16_bf16 v[64:79], a[152:155], v[184:187], v[64:79]\n"
"ds_read_b128 a[180:183], v230 offset:25600\n"
"v_add_f32 v27, v144, v27\n"
"v_exp_f32 v26, v26\n"
"v_add_f32 v237, v237, v24\n"
"v_exp_f32 v27, v27\n"
"v_add_f32 v238, v238, v25\n"
"v_mfma_f32_32x32x16_bf16 v[80:95], a[152:155], v[216:219], v[80:95]\n"
"v_cvt_pk_bf16_f32 v24, v24, v25\n"
"v_add_f32 v28, v144, v28\n"
"v_add_f32 v29, v144, v29\n"
"v_exp_f32 v28, v28\n"
"v_add_f32 v237, v237, v26\n"
"v_exp_f32 v29, v29\n"
"s_waitcnt lgkmcnt(1)\n"
"v_mfma_f32_32x32x16_bf16 v[64:79], a[156:159], v[188:191], v[64:79]\n"
"ds_read_b128 a[184:187], v231 offset:25600\n"
"s_xor_b32 s56, s86, 65536\n"
"buffer_load_dwordx4 v226, s[20:23], s56 offen offset:2048 nt lds\n"
"v_add_f32 v238, v238, v27\n"
"v_mfma_f32_32x32x16_bf16 v[80:95], a[156:159], v[220:223], v[80:95]\n"
"v_cvt_pk_bf16_f32 v25, v26, v27\n"
"v_add_f32 v30, v144, v30\n"
"v_add_f32 v31, v144, v31\n"
"v_exp_f32 v30, v30\n"
"v_add_f32 v237, v237, v28\n"
"v_exp_f32 v31, v31\n"
"v_mfma_f32_32x32x16_bf16 v[96:111], a[160:163], v[160:163], v[128:143]\n"
"ds_read_b128 a[188:191], v232 offset:25600\n"
"v_add_f32 v238, v238, v29\n"
"v_cvt_pk_bf16_f32 v26, v28, v29\n"
"v_add_f32 v32, v128, v32\n"
"v_add_f32 v33, v128, v33\n"
"v_exp_f32 v32, v32\n"
"v_add_f32 v237, v237, v30\n"
"v_mfma_f32_32x32x16_bf16 v[112:127], a[160:163], v[192:195], v[144:159]\n"
"v_exp_f32 v33, v33\n"
"v_add_f32 v238, v238, v31\n"
"v_cvt_pk_bf16_f32 v27, v30, v31\n"
"v_add_f32 v34, v128, v34\n"
"v_add_f32 v35, v128, v35\n"
"v_exp_f32 v34, v34\n"
"v_mfma_f32_32x32x16_bf16 v[96:111], a[164:167], v[164:167], v[96:111]\n"
"ds_read_b64_tr_b16 a[128:129], v233 offset:0\n"
"v_add_f32 v235, v235, v32\n"
"v_exp_f32 v35, v35\n"
"v_add_f32 v236, v236, v33\n"
"v_cvt_pk_bf16_f32 v32, v32, v33\n"
"v_add_f32 v36, v128, v36\n"
"v_add_f32 v37, v128, v37\n"
"v_mfma_f32_32x32x16_bf16 v[112:127], a[164:167], v[196:199], v[112:127]\n"
"v_max_f32 v239, v64, v65\n"
"v_max_f32 v240, v80, v81\n"
"ds_read_b64_tr_b16 a[130:131], v233 offset:2048\n"
"s_add_u32 m0, s45, 132096\n"
"s_xor_b32 s56, s86, 65664\n"
"buffer_load_dwordx4 v226, s[20:23], s56 offen nt lds\n"
"v_mfma_f32_32x32x16_bf16 v[96:111], a[168:171], v[168:171], v[96:111]\n"
"v_max3_f32 v239, v239, v66, v67\n"
"ds_read_b64_tr_b16 a[132:133], v234 offset:0\n"
"v_exp_f32 v36, v36\n"
"v_add_f32 v235, v235, v34\n"
"v_exp_f32 v37, v37\n"
"v_add_f32 v236, v236, v35\n"
"v_mfma_f32_32x32x16_bf16 v[112:127], a[168:171], v[200:203], v[112:127]\n"
"v_max3_f32 v240, v240, v82, v83\n"
"ds_read_b64_tr_b16 a[134:135], v234 offset:2048\n"
"v_cvt_pk_bf16_f32 v33, v34, v35\n"
"v_add_f32 v38, v128, v38\n"
"v_add_f32 v39, v128, v39\n"
"v_exp_f32 v38, v38\n"
"v_add_f32 v235, v235, v36\n"
"v_mfma_f32_32x32x16_bf16 v[96:111], a[172:175], v[172:175], v[96:111]\n"
"v_max3_f32 v239, v239, v68, v69\n"
"ds_read_b64_tr_b16 a[136:137], v233 offset:1024\n"
"v_exp_f32 v39, v39\n"
"v_add_f32 v236, v236, v37\n"
"v_cvt_pk_bf16_f32 v34, v36, v37\n"
"v_add_f32 v48, v144, v48\n"
"v_add_f32 v49, v144, v49\n"
"v_mfma_f32_32x32x16_bf16 v[112:127], a[172:175], v[204:207], v[112:127]\n"
"v_max3_f32 v240, v240, v84, v85\n"
"v_max3_f32 v239, v239, v70, v71\n"
"ds_read_b64_tr_b16 a[138:139], v233 offset:3072\n"
"v_exp_f32 v48, v48\n"
"v_add_f32 v235, v235, v38\n"
"v_exp_f32 v49, v49\n"
"v_mfma_f32_32x32x16_bf16 v[96:111], a[176:179], v[176:179], v[96:111]\n"
"v_max3_f32 v240, v240, v86, v87\n"
"ds_read_b64_tr_b16 a[140:141], v234 offset:1024\n"
"s_xor_b32 s56, s86, 65664\n"
"buffer_load_dwordx4 v226, s[20:23], s56 offen offset:2048 nt lds\n"
"v_mfma_f32_32x32x16_bf16 v[112:127], a[176:179], v[208:211], v[112:127]\n"
"v_max3_f32 v239, v239, v72, v73\n"
"ds_read_b64_tr_b16 a[142:143], v234 offset:3072\n"
"v_add_f32 v236, v236, v39\n"
"v_cvt_pk_bf16_f32 v35, v38, v39\n"
"v_add_f32 v50, v144, v50\n"
"v_add_f32 v51, v144, v51\n"
"v_exp_f32 v50, v50\n"
"s_waitcnt lgkmcnt(3)\n"
"v_mfma_f32_32x32x16_bf16 v[96:111], a[180:183], v[180:183], v[96:111]\n"
"v_max3_f32 v240, v240, v88, v89\n"
"ds_read_b64_tr_b16 a[144:145], v233 offset:4096\n"
"v_add_f32 v237, v237, v48\n"
"v_exp_f32 v51, v51\n"
"v_add_f32 v238, v238, v49\n"
"v_cvt_pk_bf16_f32 v48, v48, v49\n"
"v_add_f32 v52, v144, v52\n"
"v_mfma_f32_32x32x16_bf16 v[112:127], a[180:183], v[212:215], v[112:127]\n"
"v_max3_f32 v239, v239, v74, v75\n"
"v_max3_f32 v240, v240, v90, v91\n"
"ds_read_b64_tr_b16 a[146:147], v233 offset:6144\n"
"v_add_f32 v53, v144, v53\n"
"v_exp_f32 v52, v52\n"
"v_add_f32 v237, v237, v50\n"
"v_mfma_f32_32x32x16_bf16 v[96:111], a[184:187], v[184:187], v[96:111]\n"
"v_max3_f32 v239, v239, v76, v77\n"
"ds_read_b64_tr_b16 a[148:149], v234 offset:4096\n"
"v_exp_f32 v53, v53\n"
"v_add_f32 v238, v238, v51\n"
"v_cvt_pk_bf16_f32 v49, v50, v51\n"
"v_add_f32 v54, v144, v54\n"
"v_add_f32 v55, v144, v55\n"
"v_mfma_f32_32x32x16_bf16 v[112:127], a[184:187], v[216:219], v[112:127]\n"
"v_max3_f32 v240, v240, v92, v93\n"
"ds_read_b64_tr_b16 a[150:151], v234 offset:6144\n"
"v_exp_f32 v54, v54\n"
"v_add_f32 v237, v237, v52\n"
"v_exp_f32 v55, v55\n"
"v_add_f32 v238, v238, v53\n"
"v_mfma_f32_32x32x16_bf16 v[96:111], a[188:191], v[188:191], v[96:111]\n"
"v_max3_f32 v239, v239, v78, v79\n"
"ds_read_b64_tr_b16 a[152:153], v233 offset:5120\n"
"v_cvt_pk_bf16_f32 v50, v52, v53\n"
"v_add_f32 v40, v128, v40\n"
"v_add_f32 v41, v128, v41\n"
"v_exp_f32 v40, v40\n"
"v_add_f32 v237, v237, v54\n"
"v_mfma_f32_32x32x16_bf16 v[112:127], a[188:191], v[220:223], v[112:127]\n"
"v_max3_f32 v240, v240, v94, v95\n"
"ds_read_b64_tr_b16 a[154:155], v233 offset:7168\n"
"v_exp_f32 v41, v41\n"
"v_add_f32 v238, v238, v55\n"
"v_cvt_pk_bf16_f32 v51, v54, v55\n"
"v_add_f32 v42, v128, v42\n"
"v_add_f32 v43, v128, v43\n"
"v_mfma_f32_32x32x16_bf16 a[0:15], a[128:131], v[0:3], 0\n"
"ds_read_b64_tr_b16 a[156:157], v234 offset:5120\n"
"v_exp_f32 v42, v42\n"
"v_add_f32 v235, v235, v40\n"
"v_exp_f32 v43, v43\n"
"v_add_f32 v236, v236, v41\n"
"v_cvt_pk_bf16_f32 v40, v40, v41\n"
"v_mfma_f32_32x32x16_bf16 a[16:31], a[128:131], v[16:19], 0\n"
"ds_read_b64_tr_b16 a[158:159], v234 offset:7168\n"
"v_add_f32 v44, v128, v44\n"
"v_add_f32 v45, v128, v45\n"
"v_exp_f32 v44, v44\n"
"v_add_f32 v235, v235, v42\n"
"v_exp_f32 v45, v45\n"
"v_mfma_f32_32x32x16_bf16 a[32:47], a[132:135], v[0:3], 0\n"
"ds_read_b64_tr_b16 a[160:161], v233 offset:8192\n"
"v_add_f32 v236, v236, v43\n"
"v_cvt_pk_bf16_f32 v41, v42, v43\n"
"v_add_f32 v46, v128, v46\n"
"v_add_f32 v47, v128, v47\n"
"v_exp_f32 v46, v46\n"
"v_add_f32 v235, v235, v44\n"
"v_mfma_f32_32x32x16_bf16 a[48:63], a[132:135], v[16:19], 0\n"
"ds_read_b64_tr_b16 a[162:163], v233 offset:10240\n"
"v_max3_f32 v239, v239, v96, v97\n"
"v_max3_f32 v240, v240, v112, v113\n"
"v_exp_f32 v47, v47\n"
"v_add_f32 v236, v236, v45\n"
"v_cvt_pk_bf16_f32 v42, v44, v45\n"
"v_add_f32 v56, v144, v56\n"
"s_waitcnt lgkmcnt(3)\n"
"v_mfma_f32_32x32x16_bf16 a[64:79], a[136:139], v[0:3], 0\n"
"ds_read_b64_tr_b16 a[164:165], v234 offset:8192\n"
"v_max3_f32 v239, v239, v98, v99\n"
"v_max3_f32 v240, v240, v114, v115\n"
"v_add_f32 v57, v144, v57\n"
"v_exp_f32 v56, v56\n"
"v_add_f32 v235, v235, v46\n"
"v_mfma_f32_32x32x16_bf16 a[80:95], a[136:139], v[16:19], 0\n"
"ds_read_b64_tr_b16 a[166:167], v234 offset:10240\n"
"v_max3_f32 v239, v239, v100, v101\n"
"v_max3_f32 v240, v240, v116, v117\n"
"v_exp_f32 v57, v57\n"
"v_add_f32 v236, v236, v47\n"
"v_cvt_pk_bf16_f32 v43, v46, v47\n"
"v_add_f32 v58, v144, v58\n"
"v_mfma_f32_32x32x16_bf16 a[96:111], a[140:143], v[0:3], 0\n"
"ds_read_b64_tr_b16 a[168:169], v233 offset:9216\n"
"v_max3_f32 v239, v239, v102, v103\n"
"v_max3_f32 v240, v240, v118, v119\n"
"v_add_f32 v59, v144, v59\n"
"v_exp_f32 v58, v58\n"
"v_add_f32 v237, v237, v56\n"
"v_mfma_f32_32x32x16_bf16 a[112:127], a[140:143], v[16:19], 0\n"
"ds_read_b64_tr_b16 a[170:171], v233 offset:11264\n"
"v_max3_f32 v239, v239, v104, v105\n"
"v_exp_f32 v59, v59\n"
"v_add_f32 v238, v238, v57\n"
"v_cvt_pk_bf16_f32 v56, v56, v57\n"
"v_add_f32 v60, v144, v60\n"
"v_add_f32 v61, v144, v61\n"
"v_mfma_f32_32x32x16_bf16 a[0:15], a[144:147], v[8:11], a[0:15]\n"
"ds_read_b64_tr_b16 a[172:173], v234 offset:9216\n"
"v_max3_f32 v240, v240, v120, v121\n"
"v_max3_f32 v239, v239, v106, v107\n"
"v_exp_f32 v60, v60\n"
"v_add_f32 v237, v237, v58\n"
"v_exp_f32 v61, v61\n"
"v_mfma_f32_32x32x16_bf16 a[16:31], a[144:147], v[24:27], a[16:31]\n"
"v_max3_f32 v240, v240, v122, v123\n"
"v_max3_f32 v239, v239, v108, v109\n"
"v_add_f32 v238, v238, v59\n"
"v_cvt_pk_bf16_f32 v57, v58, v59\n"
"v_add_f32 v62, v144, v62\n"
"v_add_f32 v63, v144, v63\n"
"v_exp_f32 v62, v62\n"
"v_mfma_f32_32x32x16_bf16 a[32:47], a[148:151], v[8:11], a[32:47]\n"
"ds_read_b64_tr_b16 a[174:175], v234 offset:11264\n"
"v_max3_f32 v240, v240, v124, v125\n"
"v_max3_f32 v239, v239, v110, v111\n"
"v_add_f32 v237, v237, v60\n"
"v_exp_f32 v63, v63\n"
"v_add_f32 v238, v238, v61\n"
"v_cvt_pk_bf16_f32 v58, v60, v61\n"
"v_mfma_f32_32x32x16_bf16 a[48:63], a[148:151], v[24:27], a[48:63]\n"
"ds_read_b64_tr_b16 a[176:177], v233 offset:12288\n"
"v_max3_f32 v240, v240, v126, v127\n"
"v_add_f32 v237, v237, v62\n"
"v_add_f32 v238, v238, v63\n"
"v_cvt_pk_bf16_f32 v59, v62, v63\n"
"v_mfma_f32_32x32x16_bf16 a[64:79], a[152:155], v[8:11], a[64:79]\n"
"ds_read_b64_tr_b16 a[178:179], v233 offset:14336\n"
"v_max_f32 v239, v239, v240\n"
"v_cmp_lt_f32 vcc, 0x41000000, v239\n"
"s_nop 0\n"
"s_cbranch_vccnz Lstub1_%=\n"
"Lret1_%=:\n"
"v_mfma_f32_32x32x16_bf16 a[80:95], a[152:155], v[24:27], a[80:95]\n"
"ds_read_b64_tr_b16 a[180:181], v234 offset:12288\n"
"v_exp_f32 v64, v64\n"
"v_exp_f32 v65, v65\n"
"s_waitcnt lgkmcnt(4)\n"
"v_mfma_f32_32x32x16_bf16 a[96:111], a[156:159], v[8:11], a[96:111]\n"
"ds_read_b64_tr_b16 a[182:183], v234 offset:14336\n"
"v_exp_f32 v66, v66\n"
"v_add_f32 v235, v235, v64\n"
"v_exp_f32 v67, v67\n"
"v_mfma_f32_32x32x16_bf16 a[112:127], a[156:159], v[24:27], a[112:127]\n"
"ds_read_b64_tr_b16 a[184:185], v233 offset:13312\n"
"v_add_f32 v236, v236, v65\n"
"v_cvt_pk_bf16_f32 v64, v64, v65\n"
"v_exp_f32 v68, v68\n"
"v_add_f32 v235, v235, v66\n"
"v_mfma_f32_32x32x16_bf16 a[0:15], a[160:163], v[32:35], a[0:15]\n"
"ds_read_b64_tr_b16 a[186:187], v233 offset:15360\n"
"v_exp_f32 v69, v69\n"
"v_add_f32 v236, v236, v67\n"
"v_cvt_pk_bf16_f32 v65, v66, v67\n"
"v_mfma_f32_32x32x16_bf16 a[16:31], a[160:163], v[48:51], a[16:31]\n"
"ds_read_b64_tr_b16 a[188:189], v234 offset:13312\n"
"v_exp_f32 v70, v70\n"
"v_add_f32 v235, v235, v68\n"
"v_exp_f32 v71, v71\n"
"v_mfma_f32_32x32x16_bf16 a[32:47], a[164:167], v[32:35], a[32:47]\n"
"ds_read_b64_tr_b16 a[190:191], v234 offset:15360\n"
"v_add_f32 v236, v236, v69\n"
"v_cvt_pk_bf16_f32 v66, v68, v69\n"
"v_exp_f32 v80, v80\n"
"v_add_f32 v235, v235, v70\n"
"v_mfma_f32_32x32x16_bf16 a[48:63], a[164:167], v[48:51], a[48:63]\n"
"v_exp_f32 v81, v81\n"
"v_add_f32 v236, v236, v71\n"
"v_cvt_pk_bf16_f32 v67, v70, v71\n"
"v_exp_f32 v82, v82\n"
"v_mfma_f32_32x32x16_bf16 a[64:79], a[168:171], v[32:35], a[64:79]\n"
"v_add_f32 v237, v237, v80\n"
"v_exp_f32 v83, v83\n"
"v_add_f32 v238, v238, v81\n"
"v_cvt_pk_bf16_f32 v80, v80, v81\n"
"v_mfma_f32_32x32x16_bf16 a[80:95], a[168:171], v[48:51], a[80:95]\n"
"v_exp_f32 v84, v84\n"
"v_add_f32 v237, v237, v82\n"
"v_exp_f32 v85, v85\n"
"v_add_f32 v238, v238, v83\n"
"s_waitcnt lgkmcnt(1)\n"
"v_mfma_f32_32x32x16_bf16 a[96:111], a[172:175], v[32:35], a[96:111]\n"
"v_cvt_pk_bf16_f32 v81, v82, v83\n"
"v_exp_f32 v86, v86\n"
"v_add_f32 v237, v237, v84\n"
"v_exp_f32 v87, v87\n"
"v_mfma_f32_32x32x16_bf16 a[112:127], a[172:175], v[48:51], a[112:127]\n"
"v_add_f32 v238, v238, v85\n"
"v_cvt_pk_bf16_f32 v82, v84, v85\n"
"v_exp_f32 v72, v72\n"
"v_add_f32 v237, v237, v86\n"
"v_mfma_f32_32x32x16_bf16 a[0:15], a[176:179], v[40:43], a[0:15]\n"
"s_waitcnt vmcnt(17)\n"
"s_waitcnt lgkmcnt(0)\n"
"s_barrier\n"
"ds_read_b128 a[128:131], v229 offset:32768\n"
"v_exp_f32 v73, v73\n"
"v_add_f32 v238, v238, v87\n"
"v_cvt_pk_bf16_f32 v83, v86, v87\n"
"v_mfma_f32_32x32x16_bf16 a[16:31], a[176:179], v[56:59], a[16:31]\n"
"ds_read_b128 a[132:135], v230 offset:32768\n"
"s_add_u32 m0, s45, 16384\n"
"s_xor_b32 s56, s86, 81920\n"
"buffer_load_dwordx4 v224, s[16:19], s56 offen nt lds\n"
"v_mfma_f32_32x32x16_bf16 a[32:47], a[180:183], v[40:43], a[32:47]\n"
"ds_read_b128 a[136:139], v231 offset:32768\n"
"v_exp_f32 v74, v74\n"
"v_add_f32 v235, v235, v72\n"
"v_exp_f32 v75, v75\n"
"v_mfma_f32_32x32x16_bf16 a[48:63], a[180:183], v[56:59], a[48:63]\n"
"ds_read_b128 a[140:143], v232 offset:32768\n"
"v_add_f32 v236, v236, v73\n"
"v_cvt_pk_bf16_f32 v72, v72, v73\n"
"v_exp_f32 v76, v76\n"
"v_add_f32 v235, v235, v74\n"
"v_mfma_f32_32x32x16_bf16 a[64:79], a[184:187], v[40:43], a[64:79]\n"
"ds_read_b128 a[144:147], v229 offset:33792\n"
"v_exp_f32 v77, v77\n"
"v_add_f32 v236, v236, v75\n"
"v_cvt_pk_bf16_f32 v73, v74, v75\n"
"v_mfma_f32_32x32x16_bf16 a[80:95], a[184:187], v[56:59], a[80:95]\n"
"ds_read_b128 a[148:151], v230 offset:33792\n"
"v_exp_f32 v78, v78\n"
"v_add_f32 v235, v235, v76\n"
"v_exp_f32 v79, v79\n"
"v_mfma_f32_32x32x16_bf16 a[96:111], a[188:191], v[40:43], a[96:111]\n"
"ds_read_b128 a[152:155], v231 offset:33792\n"
"s_xor_b32 s56, s86, 81920\n"
"buffer_load_dwordx4 v225, s[16:19], s56 offen offset:2048 nt lds\n"
"v_mfma_f32_32x32x16_bf16 a[112:127], a[188:191], v[56:59], a[112:127]\n"
"v_add_f32 v236, v236, v77\n"
"v_cvt_pk_bf16_f32 v74, v76, v77\n"
"v_exp_f32 v88, v88\n"
"v_add_f32 v235, v235, v78\n"
"s_waitcnt lgkmcnt(3)\n"
"v_mfma_f32_32x32x16_bf16 v[0:15], a[128:131], v[160:163], v[128:143]\n"
"ds_read_b128 a[156:159], v232 offset:33792\n"
"v_exp_f32 v89, v89\n"
"v_add_f32 v236, v236, v79\n"
"v_cvt_pk_bf16_f32 v75, v78, v79\n"
"v_mfma_f32_32x32x16_bf16 v[16:31], a[128:131], v[192:195], v[144:159]\n"
"v_exp_f32 v90, v90\n"
"v_add_f32 v237, v237, v88\n"
"v_exp_f32 v91, v91\n"
"v_add_f32 v238, v238, v89\n"
"v_mfma_f32_32x32x16_bf16 v[0:15], a[132:135], v[164:167], v[0:15]\n"
"ds_read_b128 a[160:163], v229 offset:40960\n"
"v_cvt_pk_bf16_f32 v88, v88, v89\n"
"v_exp_f32 v92, v92\n"
"v_add_f32 v237, v237, v90\n"
"v_mfma_f32_32x32x16_bf16 v[16:31], a[132:135], v[196:199], v[16:31]\n"
"s_add_u32 m0, s45, 17408\n"
"s_xor_b32 s56, s86, 82048\n"
"buffer_load_dwordx4 v224, s[16:19], s56 offen nt lds\n"
"v_mfma_f32_32x32x16_bf16 v[0:15], a[136:139], v[168:171], v[0:15]\n"
"ds_read_b128 a[164:167], v230 offset:40960\n"
"s_cmp_lg_u32 s58, 0\n"
"s_cbranch_scc1 Lstub22_%=\n"
"Lret22_%=:\n"
"v_exp_f32 v93, v93\n"
"v_add_f32 v238, v238, v91\n"
"v_mfma_f32_32x32x16_bf16 v[16:31], a[136:139], v[200:203], v[16:31]\n"
"v_cvt_pk_bf16_f32 v89, v90, v91\n"
"v_exp_f32 v94, v94\n"
"v_add_f32 v237, v237, v92\n"
"v_exp_f32 v95, v95\n"
"v_mfma_f32_32x32x16_bf16 v[0:15], a[140:143], v[172:175], v[0:15]\n"
"ds_read_b128 a[168:171], v231 offset:40960\n"
"v_add_f32 v238, v238, v93\n"
"v_cvt_pk_bf16_f32 v90, v92, v93\n"
"v_exp_f32 v96, v96\n"
"v_add_f32 v237, v237, v94\n"
"v_mfma_f32_32x32x16_bf16 v[16:31], a[140:143], v[204:207], v[16:31]\n"
"v_exp_f32 v97, v97\n"
"v_add_f32 v238, v238, v95\n"
"v_cvt_pk_bf16_f32 v91, v94, v95\n"
"v_exp_f32 v98, v98\n"
"s_waitcnt lgkmcnt(2)\n"
"v_mfma_f32_32x32x16_bf16 v[0:15], a[144:147], v[176:179], v[0:15]\n"
"ds_read_b128 a[172:175], v232 offset:40960\n"
"s_xor_b32 s56, s86, 82048\n"
"buffer_load_dwordx4 v225, s[16:19], s56 offen offset:2048 nt lds\n"
"v_mfma_f32_32x32x16_bf16 v[16:31], a[144:147], v[208:211], v[16:31]\n"
"v_add_f32 v235, v235, v96\n"
"v_exp_f32 v99, v99\n"
"v_add_f32 v236, v236, v97\n"
"v_cvt_pk_bf16_f32 v96, v96, v97\n"
"v_mfma_f32_32x32x16_bf16 v[0:15], a[148:151], v[180:183], v[0:15]\n"
"ds_read_b128 a[176:179], v229 offset:41984\n"
"v_exp_f32 v100, v100\n"
"v_add_f32 v235, v235, v98\n"
"v_exp_f32 v101, v101\n"
"v_mfma_f32_32x32x16_bf16 v[16:31], a[148:151], v[212:215], v[16:31]\n"
"v_add_f32 v236, v236, v99\n"
"v_cvt_pk_bf16_f32 v97, v98, v99\n"
"v_exp_f32 v102, v102\n"
"v_add_f32 v235, v235, v100\n"
"v_mfma_f32_32x32x16_bf16 v[0:15], a[152:155], v[184:187], v[0:15]\n"
"ds_read_b128 a[180:183], v230 offset:41984\n"
"v_exp_f32 v103, v103\n"
"v_add_f32 v236, v236, v101\n"
"v_cvt_pk_bf16_f32 v98, v100, v101\n"
"v_mfma_f32_32x32x16_bf16 v[16:31], a[152:155], v[216:219], v[16:31]\n"
"s_add_u32 m0, s45, 0\n"
"s_xor_b32 s56, s86, 65536\n"
"buffer_load_dwordx4 v224, s[16:19], s56 offen nt lds\n"
"v_mfma_f32_32x32x16_bf16 v[0:15], a[156:159], v[188:191], v[0:15]\n"
"ds_read_b128 a[184:187], v231 offset:41984\n"
"v_exp_f32 v112, v112\n"
"v_add_f32 v235, v235, v102\n"
"v_exp_f32 v113, v113\n"
"v_mfma_f32_32x32x16_bf16 v[16:31], a[156:159], v[220:223], v[16:31]\n"
"v_add_f32 v236, v236, v103\n"
"v_cvt_pk_bf16_f32 v99, v102, v103\n"
"v_exp_f32 v114, v114\n"
"v_add_f32 v237, v237, v112\n"
"v_mfma_f32_32x32x16_bf16 v[32:47], a[160:163], v[160:163], v[128:143]\n"
"ds_read_b128 a[188:191], v232 offset:41984\n"
"v_exp_f32 v115, v115\n"
"v_add_f32 v238, v238, v113\n"
"v_cvt_pk_bf16_f32 v112, v112, v113\n"
"v_mfma_f32_32x32x16_bf16 v[48:63], a[160:163], v[192:195], v[144:159]\n"
"v_exp_f32 v116, v116\n"
"v_add_f32 v237, v237, v114\n"
"v_exp_f32 v117, v117\n"
"v_add_f32 v238, v238, v115\n"
"s_waitcnt lgkmcnt(2)\n"
"v_mfma_f32_32x32x16_bf16 v[32:47], a[164:167], v[164:167], v[32:47]\n"
"ds_read_b64_tr_b16 a[128:129], v233 offset:16384\n"
"s_xor_b32 s56, s86, 65536\n"
"buffer_load_dwordx4 v225, s[16:19], s56 offen offset:2048 nt lds\n"
"v_mfma_f32_32x32x16_bf16 v[48:63], a[164:167], v[196:199], v[48:63]\n"
"v_max_f32 v239, v0, v1\n"
"v_max_f32 v240, v16, v17\n"
"ds_read_b64_tr_b16 a[130:131], v233 offset:18432\n"
"v_cvt_pk_bf16_f32 v113, v114, v115\n"
"v_exp_f32 v118, v118\n"
"v_mfma_f32_32x32x16_bf16 v[32:47], a[168:171], v[168:171], v[32:47]\n"
"v_max3_f32 v239, v239, v2, v3\n"
"ds_read_b64_tr_b16 a[132:133], v234 offset:16384\n"
"v_add_f32 v237, v237, v116\n"
"v_exp_f32 v119, v119\n"
"v_add_f32 v238, v238, v117\n"
"v_mfma_f32_32x32x16_bf16 v[48:63], a[168:171], v[200:203], v[48:63]\n"
"v_max3_f32 v240, v240, v18, v19\n"
"ds_read_b64_tr_b16 a[134:135], v234 offset:18432\n"
"v_cvt_pk_bf16_f32 v114, v116, v117\n"
"v_exp_f32 v104, v104\n"
"v_add_f32 v237, v237, v118\n"
"v_mfma_f32_32x32x16_bf16 v[32:47], a[172:175], v[172:175], v[32:47]\n"
"v_max3_f32 v239, v239, v4, v5\n"
"ds_read_b64_tr_b16 a[136:137], v233 offset:17408\n"
"v_exp_f32 v105, v105\n"
"v_add_f32 v238, v238, v119\n"
"v_cvt_pk_bf16_f32 v115, v118, v119\n"
"v_mfma_f32_32x32x16_bf16 v[48:63], a[172:175], v[204:207], v[48:63]\n"
"v_max3_f32 v240, v240, v20, v21\n"
"v_max3_f32 v239, v239, v6, v7\n"
"ds_read_b64_tr_b16 a[138:139], v233 offset:19456\n"
"s_add_u32 m0, s45, 1024\n"
"s_xor_b32 s56, s86, 65664\n"
"buffer_load_dwordx4 v224, s[16:19], s56 offen nt lds\n"
"v_mfma_f32_32x32x16_bf16 v[32:47], a[176:179], v[176:179], v[32:47]\n"
"v_max3_f32 v240, v240, v22, v23\n"
"ds_read_b64_tr_b16 a[140:141], v234 offset:17408\n"
"v_exp_f32 v106, v106\n"
"v_add_f32 v235, v235, v104\n"
"v_mfma_f32_32x32x16_bf16 v[48:63], a[176:179], v[208:211], v[48:63]\n"
"v_max3_f32 v239, v239, v8, v9\n"
"ds_read_b64_tr_b16 a[142:143], v234 offset:19456\n"
"v_exp_f32 v107, v107\n"
"v_add_f32 v236, v236, v105\n"
"v_cvt_pk_bf16_f32 v104, v104, v105\n"
"v_mfma_f32_32x32x16_bf16 v[32:47], a[180:183], v[180:183], v[32:47]\n"
"v_max3_f32 v240, v240, v24, v25\n"
"ds_read_b64_tr_b16 a[144:145], v233 offset:20480\n"
"v_exp_f32 v108, v108\n"
"v_add_f32 v235, v235, v106\n"
"v_mfma_f32_32x32x16_bf16 v[48:63], a[180:183], v[212:215], v[48:63]\n"
"v_max3_f32 v239, v239, v10, v11\n"
"v_max3_f32 v240, v240, v26, v27\n"
"ds_read_b64_tr_b16 a[146:147], v233 offset:22528\n"
"v_exp_f32 v109, v109\n"
"v_add_f32 v236, v236, v107\n"
"s_waitcnt lgkmcnt(4)\n"
"v_mfma_f32_32x32x16_bf16 v[32:47], a[184:187], v[184:187], v[32:47]\n"
"v_max3_f32 v239, v239, v12, v13\n"
"ds_read_b64_tr_b16 a[148:149], v234 offset:20480\n"
"s_xor_b32 s56, s86, 65664\n"
"buffer_load_dwordx4 v225, s[16:19], s56 offen offset:2048 nt lds\n"
"v_mfma_f32_32x32x16_bf16 v[48:63], a[184:187], v[216:219], v[48:63]\n"
"v_max3_f32 v240, v240, v28, v29\n"
"ds_read_b64_tr_b16 a[150:151], v234 offset:22528\n"
"v_cvt_pk_bf16_f32 v105, v106, v107\n"
"v_exp_f32 v110, v110\n"
"v_add_f32 v235, v235, v108\n"
"v_mfma_f32_32x32x16_bf16 v[32:47], a[188:191], v[188:191], v[32:47]\n"
"v_max3_f32 v239, v239, v14, v15\n"
"ds_read_b64_tr_b16 a[152:153], v233 offset:21504\n"
"v_exp_f32 v111, v111\n"
"v_add_f32 v236, v236, v109\n"
"v_cvt_pk_bf16_f32 v106, v108, v109\n"
"v_mfma_f32_32x32x16_bf16 v[48:63], a[188:191], v[220:223], v[48:63]\n"
"v_max3_f32 v240, v240, v30, v31\n"
"ds_read_b64_tr_b16 a[154:155], v233 offset:23552\n"
"v_exp_f32 v120, v120\n"
"v_add_f32 v235, v235, v110\n"
"v_mfma_f32_32x32x16_bf16 a[0:15], a[128:131], v[64:67], a[0:15]\n"
"ds_read_b64_tr_b16 a[156:157], v234 offset:21504\n"
"v_exp_f32 v121, v121\n"
"v_add_f32 v236, v236, v111\n"
"v_cvt_pk_bf16_f32 v107, v110, v111\n"
"v_mfma_f32_32x32x16_bf16 a[16:31], a[128:131], v[80:83], a[16:31]\n"
"ds_read_b64_tr_b16 a[158:159], v234 offset:23552\n"
"s_add_u32 m0, s45, 147456\n"
"s_xor_b32 s56, s86, 81920\n"
"buffer_load_dwordx4 v226, s[20:23], s56 offen nt lds\n"
"v_mfma_f32_32x32x16_bf16 a[32:47], a[132:135], v[64:67], a[32:47]\n"
"ds_read_b64_tr_b16 a[160:161], v233 offset:24576\n"
"v_exp_f32 v122, v122\n"
"v_add_f32 v237, v237, v120\n"
"v_exp_f32 v123, v123\n"
"v_mfma_f32_32x32x16_bf16 a[48:63], a[132:135], v[80:83], a[48:63]\n"
"ds_read_b64_tr_b16 a[162:163], v233 offset:26624\n"
"v_max3_f32 v239, v239, v32, v33\n"
"v_max3_f32 v240, v240, v48, v49\n"
"v_add_f32 v238, v238, v121\n"
"v_cvt_pk_bf16_f32 v120, v120, v121\n"
"v_mfma_f32_32x32x16_bf16 a[64:79], a[136:139], v[64:67], a[64:79]\n"
"ds_read_b64_tr_b16 a[164:165], v234 offset:24576\n"
"v_max3_f32 v239, v239, v34, v35\n"
"v_max3_f32 v240, v240, v50, v51\n"
"v_exp_f32 v124, v124\n"
"v_add_f32 v237, v237, v122\n"
"v_mfma_f32_32x32x16_bf16 a[80:95], a[136:139], v[80:83], a[80:95]\n"
"ds_read_b64_tr_b16 a[166:167], v234 offset:26624\n"
"v_max3_f32 v239, v239, v36, v37\n"
"v_max3_f32 v240, v240, v52, v53\n"
"v_exp_f32 v125, v125\n"
"v_add_f32 v238, v238, v123\n"
"s_waitcnt lgkmcnt(4)\n"
"v_mfma_f32_32x32x16_bf16 a[96:111], a[140:143], v[64:67], a[96:111]\n"
"ds_read_b64_tr_b16 a[168:169], v233 offset:25600\n"
"v_max3_f32 v239, v239, v38, v39\n"
"v_max3_f32 v240, v240, v54, v55\n"
"s_xor_b32 s56, s86, 81920\n"
"buffer_load_dwordx4 v226, s[20:23], s56 offen offset:2048 nt lds\n"
"v_mfma_f32_32x32x16_bf16 a[112:127], a[140:143], v[80:83], a[112:127]\n"
"ds_read_b64_tr_b16 a[170:171], v233 offset:27648\n"
"v_max3_f32 v239, v239, v40, v41\n"
"v_cvt_pk_bf16_f32 v121, v122, v123\n"
"v_exp_f32 v126, v126\n"
"v_add_f32 v237, v237, v124\n"
"v_mfma_f32_32x32x16_bf16 a[0:15], a[144:147], v[72:75], a[0:15]\n"
"ds_read_b64_tr_b16 a[172:173], v234 offset:25600\n"
"v_max3_f32 v240, v240, v56, v57\n"
"v_max3_f32 v239, v239, v42, v43\n"
"v_exp_f32 v127, v127\n"
"v_add_f32 v238, v238, v125\n"
"v_mfma_f32_32x32x16_bf16 a[16:31], a[144:147], v[88:91], a[16:31]\n"
"v_max3_f32 v240, v240, v58, v59\n"
"v_max3_f32 v239, v239, v44, v45\n"
"v_cvt_pk_bf16_f32 v122, v124, v125\n"
"v_add_f32 v237, v237, v126\n"
"v_add_f32 v238, v238, v127\n"
"v_cvt_pk_bf16_f32 v123, v126, v127\n"
"v_mfma_f32_32x32x16_bf16 a[32:47], a[148:151], v[72:75], a[32:47]\n"
"ds_read_b64_tr_b16 a[174:175], v234 offset:27648\n"
"v_max3_f32 v240, v240, v60, v61\n"
"v_max3_f32 v239, v239, v46, v47\n"
"v_mfma_f32_32x32x16_bf16 a[48:63], a[148:151], v[88:91], a[48:63]\n"
"ds_read_b64_tr_b16 a[176:177], v233 offset:28672\n"
"v_max3_f32 v240, v240, v62, v63\n"
"s_add_u32 m0, s45, 148480\n"
"s_xor_b32 s56, s86, 82048\n"
"buffer_load_dwordx4 v226, s[20:23], s56 offen nt lds\n"
"v_mfma_f32_32x32x16_bf16 a[64:79], a[152:155], v[72:75], a[64:79]\n"
"ds_read_b64_tr_b16 a[178:179], v233 offset:30720\n"
"v_max_f32 v239, v239, v240\n"
"v_cmp_lt_f32 vcc, 0x41000000, v239\n"
"s_nop 0\n"
"s_cbranch_vccnz Lstub2_%=\n"
"Lret2_%=:\n"
"v_mfma_f32_32x32x16_bf16 a[80:95], a[152:155], v[88:91], a[80:95]\n"
"ds_read_b64_tr_b16 a[180:181], v234 offset:28672\n"
"v_exp_f32 v0, v0\n"
"v_exp_f32 v1, v1\n"
"v_mfma_f32_32x32x16_bf16 a[96:111], a[156:159], v[72:75], a[96:111]\n"
"ds_read_b64_tr_b16 a[182:183], v234 offset:30720\n"
"v_exp_f32 v2, v2\n"
"v_add_f32 v235, v235, v0\n"
"v_exp_f32 v3, v3\n"
"v_mfma_f32_32x32x16_bf16 a[112:127], a[156:159], v[88:91], a[112:127]\n"
"ds_read_b64_tr_b16 a[184:185], v233 offset:29696\n"
"v_add_f32 v236, v236, v1\n"
"v_cvt_pk_bf16_f32 v0, v0, v1\n"
"v_exp_f32 v4, v4\n"
"v_add_f32 v235, v235, v2\n"
"s_waitcnt lgkmcnt(4)\n"
"v_mfma_f32_32x32x16_bf16 a[0:15], a[160:163], v[96:99], a[0:15]\n"
"ds_read_b64_tr_b16 a[186:187], v233 offset:31744\n"
"s_xor_b32 s56, s86, 82048\n"
"buffer_load_dwordx4 v226, s[20:23], s56 offen offset:2048 nt lds\n"
"v_mfma_f32_32x32x16_bf16 a[16:31], a[160:163], v[112:115], a[16:31]\n"
"ds_read_b64_tr_b16 a[188:189], v234 offset:29696\n"
"v_exp_f32 v5, v5\n"
"v_add_f32 v236, v236, v3\n"
"v_cvt_pk_bf16_f32 v1, v2, v3\n"
"v_mfma_f32_32x32x16_bf16 a[32:47], a[164:167], v[96:99], a[32:47]\n"
"ds_read_b64_tr_b16 a[190:191], v234 offset:31744\n"
"v_exp_f32 v6, v6\n"
"v_add_f32 v235, v235, v4\n"
"v_exp_f32 v7, v7\n"
"v_mfma_f32_32x32x16_bf16 a[48:63], a[164:167], v[112:115], a[48:63]\n"
"v_add_f32 v236, v236, v5\n"
"v_cvt_pk_bf16_f32 v2, v4, v5\n"
"v_exp_f32 v16, v16\n"
"v_add_f32 v235, v235, v6\n"
"v_mfma_f32_32x32x16_bf16 a[64:79], a[168:171], v[96:99], a[64:79]\n"
"v_exp_f32 v17, v17\n"
"v_add_f32 v236, v236, v7\n"
"v_cvt_pk_bf16_f32 v3, v6, v7\n"
"v_exp_f32 v18, v18\n"
"v_mfma_f32_32x32x16_bf16 a[80:95], a[168:171], v[112:115], a[80:95]\n"
"v_add_f32 v237, v237, v16\n"
"v_exp_f32 v19, v19\n"
"v_add_f32 v238, v238, v17\n"
"v_cvt_pk_bf16_f32 v16, v16, v17\n"
"v_mfma_f32_32x32x16_bf16 a[96:111], a[172:175], v[96:99], a[96:111]\n"
"v_exp_f32 v20, v20\n"
"v_add_f32 v237, v237, v18\n"
"v_exp_f32 v21, v21\n"
"v_add_f32 v238, v238, v19\n"
"v_mfma_f32_32x32x16_bf16 a[112:127], a[172:175], v[112:115], a[112:127]\n"
"v_cvt_pk_bf16_f32 v17, v18, v19\n"
"v_exp_f32 v22, v22\n"
"v_add_f32 v237, v237, v20\n"
"v_exp_f32 v23, v23\n"
"s_waitcnt lgkmcnt(0)\n"
"v_mfma_f32_32x32x16_bf16 a[0:15], a[176:179], v[104:107], a[0:15]\n"
"s_waitcnt vmcnt(20)\n"
"s_barrier\n"
"ds_read_b128 a[128:131], v229 offset:49152\n"
"v_add_f32 v238, v238, v21\n"
"v_cvt_pk_bf16_f32 v18, v20, v21\n"
"v_exp_f32 v8, v8\n"
"v_mfma_f32_32x32x16_bf16 a[16:31], a[176:179], v[120:123], a[16:31]\n"
"ds_read_b128 a[132:135], v230 offset:49152\n"
"s_add_u32 m0, s45, 32768\n"
"s_xor_b32 s56, s86, 98304\n"
"buffer_load_dwordx4 v224, s[16:19], s56 offen nt lds\n"
"v_mfma_f32_32x32x16_bf16 a[32:47], a[180:183], v[104:107], a[32:47]\n"
"ds_read_b128 a[136:139], v231 offset:49152\n"
"v_add_f32 v237, v237, v22\n"
"v_exp_f32 v9, v9\n"
"v_add_f32 v238, v238, v23\n"
"v_cvt_pk_bf16_f32 v19, v22, v23\n"
"v_mfma_f32_32x32x16_bf16 a[48:63], a[180:183], v[120:123], a[48:63]\n"
"ds_read_b128 a[140:143], v232 offset:49152\n"
"v_exp_f32 v10, v10\n"
"v_add_f32 v235, v235, v8\n"
"v_exp_f32 v11, v11\n"
"v_mfma_f32_32x32x16_bf16 a[64:79], a[184:187], v[104:107], a[64:79]\n"
"ds_read_b128 a[144:147], v229 offset:50176\n"
"v_add_f32 v236, v236, v9\n"
"v_cvt_pk_bf16_f32 v8, v8, v9\n"
"v_exp_f32 v12, v12\n"
"v_add_f32 v235, v235, v10\n"
"v_mfma_f32_32x32x16_bf16 a[80:95], a[184:187], v[120:123], a[80:95]\n"
"ds_read_b128 a[148:151], v230 offset:50176\n"
"v_exp_f32 v13, v13\n"
"v_add_f32 v236, v236, v11\n"
"v_cvt_pk_bf16_f32 v9, v10, v11\n"
"v_mfma_f32_32x32x16_bf16 a[96:111], a[188:191], v[104:107], a[96:111]\n"
"ds_read_b128 a[152:155], v231 offset:50176\n"
"s_xor_b32 s56, s86, 98304\n"
"buffer_load_dwordx4 v225, s[16:19], s56 offen offset:2048 nt lds\n"
"v_mfma_f32_32x32x16_bf16 a[112:127], a[188:191], v[120:123], a[112:127]\n"
"v_exp_f32 v14, v14\n"
"v_add_f32 v235, v235, v12\n"
"v_exp_f32 v15, v15\n"
"v_add_f32 v236, v236, v13\n"
"s_waitcnt lgkmcnt(3)\n"
"v_mfma_f32_32x32x16_bf16 v[64:79], a[128:131], v[160:163], v[128:143]\n"
"ds_read_b128 a[156:159], v232 offset:50176\n"
"v_cvt_pk_bf16_f32 v10, v12, v13\n"
"v_exp_f32 v24, v24\n"
"v_add_f32 v235, v235, v14\n"
"v_mfma_f32_32x32x16_bf16 v[80:95], a[128:131], v[192:195], v[144:159]\n"
"v_exp_f32 v25, v25\n"
"v_add_f32 v236, v236, v15\n"
"v_cvt_pk_bf16_f32 v11, v14, v15\n"
"v_exp_f32 v26, v26\n"
"v_mfma_f32_32x32x16_bf16 v[64:79], a[132:135], v[164:167], v[64:79]\n"
"ds_read_b128 a[160:163], v229 offset:57344\n"
"v_add_f32 v237, v237, v24\n"
"v_exp_f32 v27, v27\n"
"v_add_f32 v238, v238, v25\n"
"v_cvt_pk_bf16_f32 v24, v24, v25\n"
"v_mfma_f32_32x32x16_bf16 v[80:95], a[132:135], v[196:199], v[80:95]\n"
"s_add_u32 m0, s45, 33792\n"
"s_xor_b32 s56, s86, 98432\n"
"buffer_load_dwordx4 v224, s[16:19], s56 offen nt lds\n"
"v_mfma_f32_32x32x16_bf16 v[64:79], a[136:139], v[168:171], v[64:79]\n"
"ds_read_b128 a[164:167], v230 offset:57344\n"
"s_cmp_lg_u32 s58, 0\n"
"s_cbranch_scc1 Lstub23_%=\n"
"Lret23_%=:\n"
"v_exp_f32 v28, v28\n"
"v_add_f32 v237, v237, v26\n"
"v_mfma_f32_32x32x16_bf16 v[80:95], a[136:139], v[200:203], v[80:95]\n"
"v_exp_f32 v29, v29\n"
"v_add_f32 v238, v238, v27\n"
"v_cvt_pk_bf16_f32 v25, v26, v27\n"
"v_exp_f32 v30, v30\n"
"v_mfma_f32_32x32x16_bf16 v[64:79], a[140:143], v[172:175], v[64:79]\n"
"ds_read_b128 a[168:171], v231 offset:57344\n"
"v_add_f32 v237, v237, v28\n"
"v_exp_f32 v31, v31\n"
"v_add_f32 v238, v238, v29\n"
"v_cvt_pk_bf16_f32 v26, v28, v29\n"
"v_mfma_f32_32x32x16_bf16 v[80:95], a[140:143], v[204:207], v[80:95]\n"
"v_exp_f32 v32, v32\n"
"v_add_f32 v237, v237, v30\n"
"v_exp_f32 v33, v33\n"
"v_add_f32 v238, v238, v31\n"
"s_waitcnt lgkmcnt(2)\n"
"v_mfma_f32_32x32x16_bf16 v[64:79], a[144:147], v[176:179], v[64:79]\n"
"ds_read_b128 a[172:175], v232 offset:57344\n"
"s_xor_b32 s56, s86, 98432\n"
"buffer_load_dwordx4 v225, s[16:19], s56 offen offset:2048 nt lds\n"
"v_mfma_f32_32x32x16_bf16 v[80:95], a[144:147], v[208:211], v[80:95]\n"
"v_cvt_pk_bf16_f32 v27, v30, v31\n"
"v_exp_f32 v34, v34\n"
"v_add_f32 v235, v235, v32\n"
"v_exp_f32 v35, v35\n"
"v_mfma_f32_32x32x16_bf16 v[64:79], a[148:151], v[180:183], v[64:79]\n"
"ds_read_b128 a[176:179], v229 offset:58368\n"
"v_add_f32 v236, v236, v33\n"
"v_cvt_pk_bf16_f32 v32, v32, v33\n"
"v_exp_f32 v36, v36\n"
"v_add_f32 v235, v235, v34\n"
"v_mfma_f32_32x32x16_bf16 v[80:95], a[148:151], v[212:215], v[80:95]\n"
"v_exp_f32 v37, v37\n"
"v_add_f32 v236, v236, v35\n"
"v_cvt_pk_bf16_f32 v33, v34, v35\n"
"v_exp_f32 v38, v38\n"
"v_mfma_f32_32x32x16_bf16 v[64:79], a[152:155], v[184:187], v[64:79]\n"
"ds_read_b128 a[180:183], v230 offset:58368\n"
"v_add_f32 v235, v235, v36\n"
"v_exp_f32 v39, v39\n"
"v_add_f32 v236, v236, v37\n"
"v_cvt_pk_bf16_f32 v34, v36, v37\n"
"v_mfma_f32_32x32x16_bf16 v[80:95], a[152:155], v[216:219], v[80:95]\n"
"s_add_u32 m0, s45, 65536\n"
"s_xor_b32 s56, s86, 98304\n"
"buffer_load_dwordx4 v226, s[20:23], s56 offen nt lds\n"
"v_mfma_f32_32x32x16_bf16 v[64:79], a[156:159], v[188:191], v[64:79]\n"
"ds_read_b128 a[184:187], v231 offset:58368\n"
"v_exp_f32 v48, v48\n"
"v_add_f32 v235, v235, v38\n"
"v_exp_f32 v49, v49\n"
"v_mfma_f32_32x32x16_bf16 v[80:95], a[156:159], v[220:223], v[80:95]\n"
"v_add_f32 v236, v236, v39\n"
"v_cvt_pk_bf16_f32 v35, v38, v39\n"
"v_exp_f32 v50, v50\n"
"v_add_f32 v237, v237, v48\n"
"v_mfma_f32_32x32x16_bf16 v[96:111], a[160:163], v[160:163], v[128:143]\n"
"ds_read_b128 a[188:191], v232 offset:58368\n"
"v_exp_f32 v51, v51\n"
"v_add_f32 v238, v238, v49\n"
"v_cvt_pk_bf16_f32 v48, v48, v49\n"
"v_mfma_f32_32x32x16_bf16 v[112:127], a[160:163], v[192:195], v[144:159]\n"
"v_exp_f32 v52, v52\n"
"v_add_f32 v237, v237, v50\n"
"v_exp_f32 v53, v53\n"
"v_add_f32 v238, v238, v51\n"
"s_waitcnt lgkmcnt(2)\n"
"v_mfma_f32_32x32x16_bf16 v[96:111], a[164:167], v[164:167], v[96:111]\n"
"ds_read_b64_tr_b16 a[128:129], v233 offset:32768\n"
"s_xor_b32 s56, s86, 98304\n"
"buffer_load_dwordx4 v226, s[20:23], s56 offen offset:2048 nt lds\n"
"v_mfma_f32_32x32x16_bf16 v[112:127], a[164:167], v[196:199], v[112:127]\n"
"v_max_f32 v239, v64, v65\n"
"v_max_f32 v240, v80, v81\n"
"ds_read_b64_tr_b16 a[130:131], v233 offset:34816\n"
"v_cvt_pk_bf16_f32 v49, v50, v51\n"
"v_exp_f32 v54, v54\n"
"v_mfma_f32_32x32x16_bf16 v[96:111], a[168:171], v[168:171], v[96:111]\n"
"v_max3_f32 v239, v239, v66, v67\n"
"ds_read_b64_tr_b16 a[132:133], v234 offset:32768\n"
"v_add_f32 v237, v237, v52\n"
"v_exp_f32 v55, v55\n"
"v_add_f32 v238, v238, v53\n"
"v_mfma_f32_32x32x16_bf16 v[112:127], a[168:171], v[200:203], v[112:127]\n"
"v_max3_f32 v240, v240, v82, v83\n"
"ds_read_b64_tr_b16 a[134:135], v234 offset:34816\n"
"v_cvt_pk_bf16_f32 v50, v52, v53\n"
"v_exp_f32 v40, v40\n"
"v_add_f32 v237, v237, v54\n"
"v_mfma_f32_32x32x16_bf16 v[96:111], a[172:175], v[172:175], v[96:111]\n"
"v_max3_f32 v239, v239, v68, v69\n"
"ds_read_b64_tr_b16 a[136:137], v233 offset:33792\n"
"v_exp_f32 v41, v41\n"
"v_add_f32 v238, v238, v55\n"
"v_cvt_pk_bf16_f32 v51, v54, v55\n"
"v_mfma_f32_32x32x16_bf16 v[112:127], a[172:175], v[204:207], v[112:127]\n"
"v_max3_f32 v240, v240, v84, v85\n"
"v_max3_f32 v239, v239, v70, v71\n"
"ds_read_b64_tr_b16 a[138:139], v233 offset:35840\n"
"s_add_u32 m0, s45, 66560\n"
"s_xor_b32 s56, s86, 98432\n"
"buffer_load_dwordx4 v226, s[20:23], s56 offen nt lds\n"
"v_mfma_f32_32x32x16_bf16 v[96:111], a[176:179], v[176:179], v[96:111]\n"
"v_max3_f32 v240, v240, v86, v87\n"
"ds_read_b64_tr_b16 a[140:141], v234 offset:33792\n"
"v_exp_f32 v42, v42\n"
"v_add_f32 v235, v235, v40\n"
"v_mfma_f32_32x32x16_bf16 v[112:127], a[176:179], v[208:211], v[112:127]\n"
"v_max3_f32 v239, v239, v72, v73\n"
"ds_read_b64_tr_b16 a[142:143], v234 offset:35840\n"
"v_exp_f32 v43, v43\n"
"v_add_f32 v236, v236, v41\n"
"v_cvt_pk_bf16_f32 v40, v40, v41\n"
"v_mfma_f32_32x32x16_bf16 v[96:111], a[180:183], v[180:183], v[96:111]\n"
"v_max3_f32 v240, v240, v88, v89\n"
"ds_read_b64_tr_b16 a[144:145], v233 offset:36864\n"
"v_exp_f32 v44, v44\n"
"v_add_f32 v235, v235, v42\n"
"v_mfma_f32_32x32x16_bf16 v[112:127], a[180:183], v[212:215], v[112:127]\n"
"v_max3_f32 v239, v239, v74, v75\n"
"v_max3_f32 v240, v240, v90, v91\n"
"ds_read_b64_tr_b16 a[146:147], v233 offset:38912\n"
"v_exp_f32 v45, v45\n"
"v_add_f32 v236, v236, v43\n"
"s_waitcnt lgkmcnt(4)\n"
"v_mfma_f32_32x32x16_bf16 v[96:111], a[184:187], v[184:187], v[96:111]\n"
"v_max3_f32 v239, v239, v76, v77\n"
"ds_read_b64_tr_b16 a[148:149], v234 offset:36864\n"
"s_xor_b32 s56, s86, 98432\n"
"buffer_load_dwordx4 v226, s[20:23], s56 offen offset:2048 nt lds\n"
"v_mfma_f32_32x32x16_bf16 v[112:127], a[184:187], v[216:219], v[112:127]\n"
"v_max3_f32 v240, v240, v92, v93\n"
"ds_read_b64_tr_b16 a[150:151], v234 offset:38912\n"
"v_cvt_pk_bf16_f32 v41, v42, v43\n"
"v_exp_f32 v46, v46\n"
"v_add_f32 v235, v235, v44\n"
"v_mfma_f32_32x32x16_bf16 v[96:111], a[188:191], v[188:191], v[96:111]\n"
"v_max3_f32 v239, v239, v78, v79\n"
"ds_read_b64_tr_b16 a[152:153], v233 offset:37888\n"
"v_exp_f32 v47, v47\n"
"v_add_f32 v236, v236, v45\n"
"v_cvt_pk_bf16_f32 v42, v44, v45\n"
"v_mfma_f32_32x32x16_bf16 v[112:127], a[188:191], v[220:223], v[112:127]\n"
"v_max3_f32 v240, v240, v94, v95\n"
"ds_read_b64_tr_b16 a[154:155], v233 offset:39936\n"
"v_exp_f32 v56, v56\n"
"v_add_f32 v235, v235, v46\n"
"v_mfma_f32_32x32x16_bf16 a[0:15], a[128:131], v[0:3], a[0:15]\n"
"ds_read_b64_tr_b16 a[156:157], v234 offset:37888\n"
"v_exp_f32 v57, v57\n"
"v_add_f32 v236, v236, v47\n"
"v_cvt_pk_bf16_f32 v43, v46, v47\n"
"v_mfma_f32_32x32x16_bf16 a[16:31], a[128:131], v[16:19], a[16:31]\n"
"ds_read_b64_tr_b16 a[158:159], v234 offset:39936\n"
"v_exp_f32 v58, v58\n"
"v_add_f32 v237, v237, v56\n"
"v_exp_f32 v59, v59\n"
"v_mfma_f32_32x32x16_bf16 a[32:47], a[132:135], v[0:3], a[32:47]\n"
"ds_read_b64_tr_b16 a[160:161], v233 offset:40960\n"
"v_add_f32 v238, v238, v57\n"
"v_cvt_pk_bf16_f32 v56, v56, v57\n"
"v_exp_f32 v60, v60\n"
"v_add_f32 v237, v237, v58\n"
"v_mfma_f32_32x32x16_bf16 a[48:63], a[132:135], v[16:19], a[48:63]\n"
"ds_read_b64_tr_b16 a[162:163], v233 offset:43008\n"
"v_max3_f32 v239, v239, v96, v97\n"
"v_max3_f32 v240, v240, v112, v113\n"
"v_exp_f32 v61, v61\n"
"v_add_f32 v238, v238, v59\n"
"v_mfma_f32_32x32x16_bf16 a[64:79], a[136:139], v[0:3], a[64:79]\n"
"ds_read_b64_tr_b16 a[164:165], v234 offset:40960\n"
"v_max3_f32 v239, v239, v98, v99\n"
"v_max3_f32 v240, v240, v114, v115\n"
"v_cvt_pk_bf16_f32 v57, v58, v59\n"
"v_exp_f32 v62, v62\n"
"v_mfma_f32_32x32x16_bf16 a[80:95], a[136:139], v[16:19], a[80:95]\n"
"ds_read_b64_tr_b16 a[166:167], v234 offset:43008\n"
"v_max3_f32 v239, v239, v100, v101\n"
"v_max3_f32 v240, v240, v116, v117\n"
"v_add_f32 v237, v237, v60\n"
"v_exp_f32 v63, v63\n"
"s_waitcnt lgkmcnt(4)\n"
"v_mfma_f32_32x32x16_bf16 a[96:111], a[140:143], v[0:3], a[96:111]\n"
"ds_read_b64_tr_b16 a[168:169], v233 offset:41984\n"
"v_max3_f32 v239, v239, v102, v103\n"
"v_max3_f32 v240, v240, v118, v119\n"
"v_add_f32 v238, v238, v61\n"
"v_cvt_pk_bf16_f32 v58, v60, v61\n"
"v_add_f32 v237, v237, v62\n"
"v_mfma_f32_32x32x16_bf16 a[112:127], a[140:143], v[16:19], a[112:127]\n"
"ds_read_b64_tr_b16 a[170:171], v233 offset:44032\n"
"v_max3_f32 v239, v239, v104, v105\n"
"v_add_f32 v238, v238, v63\n"
"v_cvt_pk_bf16_f32 v59, v62, v63\n"
"v_mfma_f32_32x32x16_bf16 a[0:15], a[144:147], v[8:11], a[0:15]\n"
"ds_read_b64_tr_b16 a[172:173], v234 offset:41984\n"
"v_max3_f32 v240, v240, v120, v121\n"
"v_max3_f32 v239, v239, v106, v107\n"
"v_mfma_f32_32x32x16_bf16 a[16:31], a[144:147], v[24:27], a[16:31]\n"
"v_max3_f32 v240, v240, v122, v123\n"
"v_max3_f32 v239, v239, v108, v109\n"
"v_mfma_f32_32x32x16_bf16 a[32:47], a[148:151], v[8:11], a[32:47]\n"
"ds_read_b64_tr_b16 a[174:175], v234 offset:44032\n"
"v_max3_f32 v240, v240, v124, v125\n"
"v_max3_f32 v239, v239, v110, v111\n"
"v_mfma_f32_32x32x16_bf16 a[48:63], a[148:151], v[24:27], a[48:63]\n"
"ds_read_b64_tr_b16 a[176:177], v233 offset:45056\n"
"v_max3_f32 v240, v240, v126, v127\n"
"v_mfma_f32_32x32x16_bf16 a[64:79], a[152:155], v[8:11], a[64:79]\n"
"ds_read_b64_tr_b16 a[178:179], v233 offset:47104\n"
"v_max_f32 v239, v239, v240\n"
"v_cmp_lt_f32 vcc, 0x41000000, v239\n"
"s_nop 0\n"
"s_cbranch_vccnz Lstub3_%=\n"
"Lret3_%=:\n"
"v_mfma_f32_32x32x16_bf16 a[80:95], a[152:155], v[24:27], a[80:95]\n"
"ds_read_b64_tr_b16 a[180:181], v234 offset:45056\n"
"v_exp_f32 v64, v64\n"
"v_exp_f32 v65, v65\n"
"v_exp_f32 v66, v66\n"
"v_mfma_f32_32x32x16_bf16 a[96:111], a[156:159], v[8:11], a[96:111]\n"
"ds_read_b64_tr_b16 a[182:183], v234 offset:47104\n"
"v_add_f32 v235, v235, v64\n"
"v_exp_f32 v67, v67\n"
"v_add_f32 v236, v236, v65\n"
"v_cvt_pk_bf16_f32 v64, v64, v65\n"
"v_mfma_f32_32x32x16_bf16 a[112:127], a[156:159], v[24:27], a[112:127]\n"
"ds_read_b64_tr_b16 a[184:185], v233 offset:46080\n"
"v_exp_f32 v68, v68\n"
"v_add_f32 v235, v235, v66\n"
"v_exp_f32 v69, v69\n"
"v_add_f32 v236, v236, v67\n"
"s_waitcnt lgkmcnt(4)\n"
"v_mfma_f32_32x32x16_bf16 a[0:15], a[160:163], v[32:35], a[0:15]\n"
"ds_read_b64_tr_b16 a[186:187], v233 offset:48128\n"
"v_cvt_pk_bf16_f32 v65, v66, v67\n"
"v_exp_f32 v70, v70\n"
"v_add_f32 v235, v235, v68\n"
"v_exp_f32 v71, v71\n"
"v_mfma_f32_32x32x16_bf16 a[16:31], a[160:163], v[48:51], a[16:31]\n"
"ds_read_b64_tr_b16 a[188:189], v234 offset:46080\n"
"v_add_f32 v236, v236, v69\n"
"v_cvt_pk_bf16_f32 v66, v68, v69\n"
"v_exp_f32 v80, v80\n"
"v_add_f32 v235, v235, v70\n"
"v_mfma_f32_32x32x16_bf16 a[32:47], a[164:167], v[32:35], a[32:47]\n"
"ds_read_b64_tr_b16 a[190:191], v234 offset:48128\n"
"v_exp_f32 v81, v81\n"
"v_add_f32 v236, v236, v71\n"
"v_cvt_pk_bf16_f32 v67, v70, v71\n"
"v_exp_f32 v82, v82\n"
"v_mfma_f32_32x32x16_bf16 a[48:63], a[164:167], v[48:51], a[48:63]\n"
"v_add_f32 v237, v237, v80\n"
"v_exp_f32 v83, v83\n"
"v_add_f32 v238, v238, v81\n"
"v_cvt_pk_bf16_f32 v80, v80, v81\n"
"v_exp_f32 v84, v84\n"
"v_mfma_f32_32x32x16_bf16 a[64:79], a[168:171], v[32:35], a[64:79]\n"
"v_add_f32 v237, v237, v82\n"
"v_exp_f32 v85, v85\n"
"v_add_f32 v238, v238, v83\n"
"v_cvt_pk_bf16_f32 v81, v82, v83\n"
"v_exp_f32 v86, v86\n"
"v_mfma_f32_32x32x16_bf16 a[80:95], a[168:171], v[48:51], a[80:95]\n"
"v_add_f32 v237, v237, v84\n"
"v_exp_f32 v87, v87\n"
"v_add_f32 v238, v238, v85\n"
"v_cvt_pk_bf16_f32 v82, v84, v85\n"
"v_exp_f32 v72, v72\n"
"v_mfma_f32_32x32x16_bf16 a[96:111], a[172:175], v[32:35], a[96:111]\n"
"v_add_f32 v237, v237, v86\n"
"v_exp_f32 v73, v73\n"
"v_add_f32 v238, v238, v87\n"
"v_cvt_pk_bf16_f32 v83, v86, v87\n"
"v_exp_f32 v74, v74\n"
"v_mfma_f32_32x32x16_bf16 a[112:127], a[172:175], v[48:51], a[112:127]\n"
"v_add_f32 v235, v235, v72\n"
"v_exp_f32 v75, v75\n"
"v_add_f32 v236, v236, v73\n"
"v_cvt_pk_bf16_f32 v72, v72, v73\n"
"v_exp_f32 v76, v76\n"
"s_waitcnt lgkmcnt(0)\n"
"v_mfma_f32_32x32x16_bf16 a[0:15], a[176:179], v[40:43], a[0:15]\n"
"s_waitcnt vmcnt(12)\n"
"s_barrier\n"
"ds_read_b128 a[128:131], v229 offset:0\n"
"v_add_f32 v235, v235, v74\n"
"v_exp_f32 v77, v77\n"
"v_add_f32 v236, v236, v75\n"
"v_cvt_pk_bf16_f32 v73, v74, v75\n"
"v_mfma_f32_32x32x16_bf16 a[16:31], a[176:179], v[56:59], a[16:31]\n"
"ds_read_b128 a[132:135], v230 offset:0\n"
"s_add_u32 m0, s45, 49152\n"
"s_xor_b32 s56, s86, 114688\n"
"buffer_load_dwordx4 v224, s[16:19], s56 offen nt lds\n"
"v_mfma_f32_32x32x16_bf16 a[32:47], a[180:183], v[40:43], a[32:47]\n"
"ds_read_b128 a[136:139], v231 offset:0\n"
"v_exp_f32 v78, v78\n"
"v_add_f32 v235, v235, v76\n"
"v_exp_f32 v79, v79\n"
"v_add_f32 v236, v236, v77\n"
"v_mfma_f32_32x32x16_bf16 a[48:63], a[180:183], v[56:59], a[48:63]\n"
"ds_read_b128 a[140:143], v232 offset:0\n"
"v_cvt_pk_bf16_f32 v74, v76, v77\n"
"v_exp_f32 v88, v88\n"
"v_add_f32 v235, v235, v78\n"
"v_exp_f32 v89, v89\n"
"v_mfma_f32_32x32x16_bf16 a[64:79], a[184:187], v[40:43], a[64:79]\n"
"ds_read_b128 a[144:147], v229 offset:1024\n"
"v_add_f32 v236, v236, v79\n"
"v_cvt_pk_bf16_f32 v75, v78, v79\n"
"v_exp_f32 v90, v90\n"
"v_add_f32 v237, v237, v88\n"
"v_mfma_f32_32x32x16_bf16 a[80:95], a[184:187], v[56:59], a[80:95]\n"
"ds_read_b128 a[148:151], v230 offset:1024\n"
"v_exp_f32 v91, v91\n"
"v_add_f32 v238, v238, v89\n"
"v_cvt_pk_bf16_f32 v88, v88, v89\n"
"v_exp_f32 v92, v92\n"
"v_mfma_f32_32x32x16_bf16 a[96:111], a[188:191], v[40:43], a[96:111]\n"
"ds_read_b128 a[152:155], v231 offset:1024\n"
"s_xor_b32 s56, s86, 114688\n"
"buffer_load_dwordx4 v225, s[16:19], s56 offen offset:2048 nt lds\n"
"v_mfma_f32_32x32x16_bf16 a[112:127], a[188:191], v[56:59], a[112:127]\n"
"v_add_f32 v237, v237, v90\n"
"v_exp_f32 v93, v93\n"
"v_add_f32 v238, v238, v91\n"
"v_cvt_pk_bf16_f32 v89, v90, v91\n"
"v_exp_f32 v94, v94\n"
"s_waitcnt lgkmcnt(3)\n"
"v_mfma_f32_32x32x16_bf16 v[0:15], a[128:131], v[160:163], v[128:143]\n"
"ds_read_b128 a[156:159], v232 offset:1024\n"
"v_add_f32 v237, v237, v92\n"
"v_exp_f32 v95, v95\n"
"v_add_f32 v238, v238, v93\n"
"v_cvt_pk_bf16_f32 v90, v92, v93\n"
"v_mfma_f32_32x32x16_bf16 v[16:31], a[128:131], v[192:195], v[144:159]\n"
"v_exp_f32 v96, v96\n"
"v_add_f32 v237, v237, v94\n"
"v_exp_f32 v97, v97\n"
"v_add_f32 v238, v238, v95\n"
"v_cvt_pk_bf16_f32 v91, v94, v95\n"
"v_mfma_f32_32x32x16_bf16 v[0:15], a[132:135], v[164:167], v[0:15]\n"
"ds_read_b128 a[160:163], v229 offset:8192\n"
"v_exp_f32 v98, v98\n"
"v_add_f32 v235, v235, v96\n"
"v_exp_f32 v99, v99\n"
"v_add_f32 v236, v236, v97\n"
"v_mfma_f32_32x32x16_bf16 v[16:31], a[132:135], v[196:199], v[16:31]\n"
"s_add_u32 m0, s45, 50176\n"
"s_xor_b32 s56, s86, 114816\n"
"buffer_load_dwordx4 v224, s[16:19], s56 offen nt lds\n"
"v_cvt_pk_bf16_f32 v96, v96, v97\n"
"v_mfma_f32_32x32x16_bf16 v[0:15], a[136:139], v[168:171], v[0:15]\n"
"ds_read_b128 a[164:167], v230 offset:8192\n"
"s_cmp_lg_u32 s58, 0\n"
"s_cbranch_scc1 Lstub24_%=\n"
"Lret24_%=:\n"
"v_exp_f32 v100, v100\n"
"v_add_f32 v235, v235, v98\n"
"v_mfma_f32_32x32x16_bf16 v[16:31], a[136:139], v[200:203], v[16:31]\n"
"v_exp_f32 v101, v101\n"
"v_add_f32 v236, v236, v99\n"
"v_cvt_pk_bf16_f32 v97, v98, v99\n"
"v_exp_f32 v102, v102\n"
"v_add_f32 v235, v235, v100\n"
"v_mfma_f32_32x32x16_bf16 v[0:15], a[140:143], v[172:175], v[0:15]\n"
"ds_read_b128 a[168:171], v231 offset:8192\n"
"v_exp_f32 v103, v103\n"
"v_add_f32 v236, v236, v101\n"
"v_cvt_pk_bf16_f32 v98, v100, v101\n"
"v_exp_f32 v112, v112\n"
"v_mfma_f32_32x32x16_bf16 v[16:31], a[140:143], v[204:207], v[16:31]\n"
"v_add_f32 v235, v235, v102\n"
"v_exp_f32 v113, v113\n"
"v_add_f32 v236, v236, v103\n"
"v_cvt_pk_bf16_f32 v99, v102, v103\n"
"v_exp_f32 v114, v114\n"
"s_waitcnt lgkmcnt(2)\n"
"v_mfma_f32_32x32x16_bf16 v[0:15], a[144:147], v[176:179], v[0:15]\n"
"ds_read_b128 a[172:175], v232 offset:8192\n"
"s_xor_b32 s56, s86, 114816\n"
"buffer_load_dwordx4 v225, s[16:19], s56 offen offset:2048 nt lds\n"
"v_mfma_f32_32x32x16_bf16 v[16:31], a[144:147], v[208:211], v[16:31]\n"
"v_add_f32 v237, v237, v112\n"
"v_exp_f32 v115, v115\n"
"v_add_f32 v238, v238, v113\n"
"v_cvt_pk_bf16_f32 v112, v112, v113\n"
"v_exp_f32 v116, v116\n"
"v_mfma_f32_32x32x16_bf16 v[0:15], a[148:151], v[180:183], v[0:15]\n"
"ds_read_b128 a[176:179], v229 offset:9216\n"
"v_add_f32 v237, v237, v114\n"
"v_exp_f32 v117, v117\n"
"v_add_f32 v238, v238, v115\n"
"v_cvt_pk_bf16_f32 v113, v114, v115\n"
"v_mfma_f32_32x32x16_bf16 v[16:31], a[148:151], v[212:215], v[16:31]\n"
"v_exp_f32 v118, v118\n"
"v_add_f32 v237, v237, v116\n"
"v_exp_f32 v119, v119\n"
"v_add_f32 v238, v238, v117\n"
"v_cvt_pk_bf16_f32 v114, v116, v117\n"
"v_mfma_f32_32x32x16_bf16 v[0:15], a[152:155], v[184:187], v[0:15]\n"
"ds_read_b128 a[180:183], v230 offset:9216\n"
"v_exp_f32 v104, v104\n"
"v_add_f32 v237, v237, v118\n"
"v_exp_f32 v105, v105\n"
"v_add_f32 v238, v238, v119\n"
"v_mfma_f32_32x32x16_bf16 v[16:31], a[152:155], v[216:219], v[16:31]\n"
"s_add_u32 m0, s45, 81920\n"
"s_xor_b32 s56, s86, 114688\n"
"buffer_load_dwordx4 v226, s[20:23], s56 offen nt lds\n"
"v_cvt_pk_bf16_f32 v115, v118, v119\n"
"v_mfma_f32_32x32x16_bf16 v[0:15], a[156:159], v[188:191], v[0:15]\n"
"ds_read_b128 a[184:187], v231 offset:9216\n"
"v_exp_f32 v106, v106\n"
"v_add_f32 v235, v235, v104\n"
"v_exp_f32 v107, v107\n"
"v_add_f32 v236, v236, v105\n"
"v_mfma_f32_32x32x16_bf16 v[16:31], a[156:159], v[220:223], v[16:31]\n"
"v_cvt_pk_bf16_f32 v104, v104, v105\n"
"v_exp_f32 v108, v108\n"
"v_add_f32 v235, v235, v106\n"
"v_exp_f32 v109, v109\n"
"v_add_f32 v236, v236, v107\n"
"v_mfma_f32_32x32x16_bf16 v[32:47], a[160:163], v[160:163], v[128:143]\n"
"ds_read_b128 a[188:191], v232 offset:9216\n"
"v_cvt_pk_bf16_f32 v105, v106, v107\n"
"v_exp_f32 v110, v110\n"
"v_add_f32 v235, v235, v108\n"
"v_exp_f32 v111, v111\n"
"v_mfma_f32_32x32x16_bf16 v[48:63], a[160:163], v[192:195], v[144:159]\n"
"v_add_f32 v236, v236, v109\n"
"v_cvt_pk_bf16_f32 v106, v108, v109\n"
"v_exp_f32 v120, v120\n"
"v_add_f32 v235, v235, v110\n"
"v_exp_f32 v121, v121\n"
"s_waitcnt lgkmcnt(2)\n"
"v_mfma_f32_32x32x16_bf16 v[32:47], a[164:167], v[164:167], v[32:47]\n"
"ds_read_b64_tr_b16 a[128:129], v233 offset:49152\n"
"s_xor_b32 s56, s86, 114688\n"
"buffer_load_dwordx4 v226, s[20:23], s56 offen offset:2048 nt lds\n"
"v_mfma_f32_32x32x16_bf16 v[48:63], a[164:167], v[196:199], v[48:63]\n"
"v_max_f32 v239, v0, v1\n"
"v_max_f32 v240, v16, v17\n"
"ds_read_b64_tr_b16 a[130:131], v233 offset:51200\n"
"v_add_f32 v236, v236, v111\n"
"v_cvt_pk_bf16_f32 v107, v110, v111\n"
"v_exp_f32 v122, v122\n"
"v_mfma_f32_32x32x16_bf16 v[32:47], a[168:171], v[168:171], v[32:47]\n"
"v_max3_f32 v239, v239, v2, v3\n"
"ds_read_b64_tr_b16 a[132:133], v234 offset:49152\n"
"v_add_f32 v237, v237, v120\n"
"v_exp_f32 v123, v123\n"
"v_add_f32 v238, v238, v121\n"
"v_cvt_pk_bf16_f32 v120, v120, v121\n"
"v_mfma_f32_32x32x16_bf16 v[48:63], a[168:171], v[200:203], v[48:63]\n"
"v_max3_f32 v240, v240, v18, v19\n"
"ds_read_b64_tr_b16 a[134:135], v234 offset:51200\n"
"v_exp_f32 v124, v124\n"
"v_add_f32 v237, v237, v122\n"
"v_exp_f32 v125, v125\n"
"v_mfma_f32_32x32x16_bf16 v[32:47], a[172:175], v[172:175], v[32:47]\n"
"v_max3_f32 v239, v239, v4, v5\n"
"ds_read_b64_tr_b16 a[136:137], v233 offset:50176\n"
"v_add_f32 v238, v238, v123\n"
"v_cvt_pk_bf16_f32 v121, v122, v123\n"
"v_exp_f32 v126, v126\n"
"v_add_f32 v237, v237, v124\n"
"v_mfma_f32_32x32x16_bf16 v[48:63], a[172:175], v[204:207], v[48:63]\n"
"v_max3_f32 v240, v240, v20, v21\n"
"v_max3_f32 v239, v239, v6, v7\n"
"ds_read_b64_tr_b16 a[138:139], v233 offset:52224\n"
"s_add_u32 m0, s45, 82944\n"
"s_xor_b32 s56, s86, 114816\n"
"buffer_load_dwordx4 v226, s[20:23], s56 offen nt lds\n"
"v_mfma_f32_32x32x16_bf16 v[32:47], a[176:179], v[176:179], v[32:47]\n"
"v_max3_f32 v240, v240, v22, v23\n"
"ds_read_b64_tr_b16 a[140:141], v234 offset:50176\n"
"v_exp_f32 v127, v127\n"
"v_add_f32 v238, v238, v125\n"
"v_cvt_pk_bf16_f32 v122, v124, v125\n"
"v_add_f32 v237, v237, v126\n"
"v_mfma_f32_32x32x16_bf16 v[48:63], a[176:179], v[208:211], v[48:63]\n"
"v_max3_f32 v239, v239, v8, v9\n"
"ds_read_b64_tr_b16 a[142:143], v234 offset:52224\n"
"v_add_f32 v238, v238, v127\n"
"v_cvt_pk_bf16_f32 v123, v126, v127\n"
"v_mfma_f32_32x32x16_bf16 v[32:47], a[180:183], v[180:183], v[32:47]\n"
"v_max3_f32 v240, v240, v24, v25\n"
"ds_read_b64_tr_b16 a[144:145], v233 offset:53248\n"
"v_mfma_f32_32x32x16_bf16 v[48:63], a[180:183], v[212:215], v[48:63]\n"
"v_max3_f32 v239, v239, v10, v11\n"
"v_max3_f32 v240, v240, v26, v27\n"
"ds_read_b64_tr_b16 a[146:147], v233 offset:55296\n"
"s_waitcnt lgkmcnt(5)\n"
"v_mfma_f32_32x32x16_bf16 v[32:47], a[184:187], v[184:187], v[32:47]\n"
"v_max3_f32 v239, v239, v12, v13\n"
"ds_read_b64_tr_b16 a[148:149], v234 offset:53248\n"
"s_xor_b32 s56, s86, 114816\n"
"buffer_load_dwordx4 v226, s[20:23], s56 offen offset:2048 nt lds\n"
"v_mfma_f32_32x32x16_bf16 v[48:63], a[184:187], v[216:219], v[48:63]\n"
"v_max3_f32 v240, v240, v28, v29\n"
"ds_read_b64_tr_b16 a[150:151], v234 offset:55296\n"
"v_add_u32 v226, s45, v233\n"
"v_mfma_f32_32x32x16_bf16 v[32:47], a[188:191], v[188:191], v[32:47]\n"
"v_max3_f32 v239, v239, v14, v15\n"
"ds_read_b64_tr_b16 a[152:153], v233 offset:54272\n"
"v_add_u32 v251, s45, v253\n"
"v_mfma_f32_32x32x16_bf16 v[48:63], a[188:191], v[220:223], v[48:63]\n"
"v_max3_f32 v240, v240, v30, v31\n"
"ds_read_b64_tr_b16 a[154:155], v233 offset:56320\n"
"v_subrev_u32 v224, 0x10000, v226\n"
"v_mfma_f32_32x32x16_bf16 a[0:15], a[128:131], v[64:67], a[0:15]\n"
"ds_read_b64_tr_b16 a[156:157], v234 offset:54272\n"
"v_add_u32 v250, s45, v234\n"
"v_mfma_f32_32x32x16_bf16 a[16:31], a[128:131], v[80:83], a[16:31]\n"
"ds_read_b64_tr_b16 a[158:159], v234 offset:56320\n"
"v_add_u32 v255, s45, v254\n"
"v_mfma_f32_32x32x16_bf16 a[32:47], a[132:135], v[64:67], a[32:47]\n"
"ds_read_b64_tr_b16 a[160:161], v233 offset:57344\n"
"v_subrev_u32 v225, 0x10000, v250\n"
"s_add_u32 m0, s45, 98304\n"
"s_add_u32 s56, s49, 0\n"
"s_and_b32 s56, s56, 0xfffff\n"
"buffer_load_dwordx4 v227, s[24:27], s56 offen lds\n"
"v_mfma_f32_32x32x16_bf16 a[48:63], a[132:135], v[80:83], a[48:63]\n"
"ds_read_b64_tr_b16 a[162:163], v233 offset:59392\n"
"v_max3_f32 v239, v239, v32, v33\n"
"v_max3_f32 v240, v240, v48, v49\n"
"buffer_load_dwordx4 v252, s[24:27], s56 offen offset:1024 lds\n"
"s_waitcnt lgkmcnt(5)\n"
"v_mfma_f32_32x32x16_bf16 a[64:79], a[136:139], v[64:67], a[64:79]\n"
"ds_read_b64_tr_b16 a[164:165], v234 offset:57344\n"
"v_max3_f32 v239, v239, v34, v35\n"
"v_max3_f32 v240, v240, v50, v51\n"
"v_mfma_f32_32x32x16_bf16 a[80:95], a[136:139], v[80:83], a[80:95]\n"
"ds_read_b64_tr_b16 a[166:167], v234 offset:59392\n"
"v_max3_f32 v239, v239, v36, v37\n"
"v_max3_f32 v240, v240, v52, v53\n"
"v_mfma_f32_32x32x16_bf16 a[96:111], a[140:143], v[64:67], a[96:111]\n"
"ds_read_b64_tr_b16 a[168:169], v233 offset:58368\n"
"v_max3_f32 v239, v239, v38, v39\n"
"v_max3_f32 v240, v240, v54, v55\n"
"v_mfma_f32_32x32x16_bf16 a[112:127], a[140:143], v[80:83], a[112:127]\n"
"ds_read_b64_tr_b16 a[170:171], v233 offset:60416\n"
"v_max3_f32 v239, v239, v40, v41\n"
"v_mfma_f32_32x32x16_bf16 a[0:15], a[144:147], v[72:75], a[0:15]\n"
"ds_read_b64_tr_b16 a[172:173], v234 offset:58368\n"
"v_max3_f32 v240, v240, v56, v57\n"
"v_max3_f32 v239, v239, v42, v43\n"
"s_add_u32 m0, s45, 100352\n"
"s_add_u32 s56, s49, 16384\n"
"s_and_b32 s56, s56, 0xfffff\n"
"buffer_load_dwordx4 v227, s[24:27], s56 offen lds\n"
"v_mfma_f32_32x32x16_bf16 a[16:31], a[144:147], v[88:91], a[16:31]\n"
"v_max3_f32 v240, v240, v58, v59\n"
"v_max3_f32 v239, v239, v44, v45\n"
"buffer_load_dwordx4 v252, s[24:27], s56 offen offset:1024 lds\n"
"v_mfma_f32_32x32x16_bf16 a[32:47], a[148:151], v[72:75], a[32:47]\n"
"ds_read_b64_tr_b16 a[174:175], v234 offset:60416\n"
"v_max3_f32 v240, v240, v60, v61\n"
"v_max3_f32 v239, v239, v46, v47\n"
"v_mfma_f32_32x32x16_bf16 a[48:63], a[148:151], v[88:91], a[48:63]\n"
"ds_read_b64_tr_b16 a[176:177], v233 offset:61440\n"
"v_max3_f32 v240, v240, v62, v63\n"
"s_waitcnt lgkmcnt(4)\n"
"v_mfma_f32_32x32x16_bf16 a[64:79], a[152:155], v[72:75], a[64:79]\n"
"ds_read_b64_tr_b16 a[178:179], v233 offset:63488\n"
"v_max_f32 v239, v239, v240\n"
"v_cmp_lt_f32 vcc, 0x41000000, v239\n"
"s_nop 0\n"
"s_cbranch_vccnz Lstub4_%=\n"
"Lret4_%=:\n"
"v_mfma_f32_32x32x16_bf16 a[80:95], a[152:155], v[88:91], a[80:95]\n"
"ds_read_b64_tr_b16 a[180:181], v234 offset:61440\n"
"v_exp_f32 v0, v0\n"
"v_exp_f32 v1, v1\n"
"v_exp_f32 v2, v2\n"
"v_mfma_f32_32x32x16_bf16 a[96:111], a[156:159], v[72:75], a[96:111]\n"
"ds_read_b64_tr_b16 a[182:183], v234 offset:63488\n"
"v_add_f32 v235, v235, v0\n"
"v_exp_f32 v3, v3\n"
"v_add_f32 v236, v236, v1\n"
"v_cvt_pk_bf16_f32 v0, v0, v1\n"
"v_mfma_f32_32x32x16_bf16 a[112:127], a[156:159], v[88:91], a[112:127]\n"
"ds_read_b64_tr_b16 a[184:185], v233 offset:62464\n"
"v_exp_f32 v4, v4\n"
"v_add_f32 v235, v235, v2\n"
"v_exp_f32 v5, v5\n"
"v_add_f32 v236, v236, v3\n"
"v_mfma_f32_32x32x16_bf16 a[0:15], a[160:163], v[96:99], a[0:15]\n"
"ds_read_b64_tr_b16 a[186:187], v233 offset:64512\n"
"v_cvt_pk_bf16_f32 v1, v2, v3\n"
"v_exp_f32 v6, v6\n"
"v_add_f32 v235, v235, v4\n"
"v_exp_f32 v7, v7\n"
"v_mfma_f32_32x32x16_bf16 a[16:31], a[160:163], v[112:115], a[16:31]\n"
"ds_read_b64_tr_b16 a[188:189], v234 offset:62464\n"
"v_add_f32 v236, v236, v5\n"
"v_cvt_pk_bf16_f32 v2, v4, v5\n"
"v_exp_f32 v16, v16\n"
"v_add_f32 v235, v235, v6\n"
"v_mfma_f32_32x32x16_bf16 a[32:47], a[164:167], v[96:99], a[32:47]\n"
"ds_read_b64_tr_b16 a[190:191], v234 offset:64512\n"
"v_exp_f32 v17, v17\n"
"v_add_f32 v236, v236, v7\n"
"v_cvt_pk_bf16_f32 v3, v6, v7\n"
"v_exp_f32 v18, v18\n"
"v_mfma_f32_32x32x16_bf16 a[48:63], a[164:167], v[112:115], a[48:63]\n"
"v_add_f32 v237, v237, v16\n"
"v_exp_f32 v19, v19\n"
"v_add_f32 v238, v238, v17\n"
"v_cvt_pk_bf16_f32 v16, v16, v17\n"
"v_exp_f32 v20, v20\n"
"s_waitcnt lgkmcnt(3)\n"
"v_mfma_f32_32x32x16_bf16 a[64:79], a[168:171], v[96:99], a[64:79]\n"
"v_add_f32 v237, v237, v18\n"
"v_exp_f32 v21, v21\n"
"v_add_f32 v238, v238, v19\n"
"v_cvt_pk_bf16_f32 v17, v18, v19\n"
"v_exp_f32 v22, v22\n"
"v_mfma_f32_32x32x16_bf16 a[80:95], a[168:171], v[112:115], a[80:95]\n"
"v_add_f32 v237, v237, v20\n"
"v_exp_f32 v23, v23\n"
"v_add_f32 v238, v238, v21\n"
"v_cvt_pk_bf16_f32 v18, v20, v21\n"
"v_exp_f32 v8, v8\n"
"v_mfma_f32_32x32x16_bf16 a[96:111], a[172:175], v[96:99], a[96:111]\n"
"v_add_f32 v237, v237, v22\n"
"v_exp_f32 v9, v9\n"
"v_add_f32 v238, v238, v23\n"
"v_cvt_pk_bf16_f32 v19, v22, v23\n"
"v_exp_f32 v10, v10\n"
"v_mfma_f32_32x32x16_bf16 a[112:127], a[172:175], v[112:115], a[112:127]\n"
"v_add_f32 v235, v235, v8\n"
"v_exp_f32 v11, v11\n"
"v_add_f32 v236, v236, v9\n"
"v_cvt_pk_bf16_f32 v8, v8, v9\n"
"v_exp_f32 v12, v12\n"
"v_mfma_f32_32x32x16_bf16 a[0:15], a[176:179], v[104:107], a[0:15]\n"
"s_waitcnt lgkmcnt(0)\n"
"s_barrier\n"
"ds_read_b128 a[128:131], v229 offset:16384\n"
"v_add_f32 v235, v235, v10\n"
"v_exp_f32 v13, v13\n"
"v_add_f32 v236, v236, v11\n"
"v_cvt_pk_bf16_f32 v9, v10, v11\n"
"v_mfma_f32_32x32x16_bf16 a[16:31], a[176:179], v[120:123], a[16:31]\n"
"ds_read_b128 a[132:135], v230 offset:16384\n"
"s_add_u32 m0, s45, 0\n"
"s_add_u32 s56, s49, 32768\n"
"s_and_b32 s56, s56, 0xfffff\n"
"buffer_load_dwordx4 v227, s[24:27], s56 offen lds\n"
"v_mfma_f32_32x32x16_bf16 a[32:47], a[180:183], v[104:107], a[32:47]\n"
"ds_read_b128 a[136:139], v231 offset:16384\n"
"buffer_load_dwordx4 v252, s[24:27], s56 offen offset:1024 lds\n"
"v_exp_f32 v14, v14\n"
"v_mfma_f32_32x32x16_bf16 a[48:63], a[180:183], v[120:123], a[48:63]\n"
"ds_read_b128 a[140:143], v232 offset:16384\n"
"v_add_f32 v235, v235, v12\n"
"v_exp_f32 v15, v15\n"
"v_add_f32 v236, v236, v13\n"
"v_cvt_pk_bf16_f32 v10, v12, v13\n"
"v_mfma_f32_32x32x16_bf16 a[64:79], a[184:187], v[104:107], a[64:79]\n"
"ds_read_b128 a[144:147], v229 offset:17408\n"
"v_exp_f32 v24, v24\n"
"v_add_f32 v235, v235, v14\n"
"v_exp_f32 v25, v25\n"
"v_add_f32 v236, v236, v15\n"
"v_mfma_f32_32x32x16_bf16 a[80:95], a[184:187], v[120:123], a[80:95]\n"
"ds_read_b128 a[148:151], v230 offset:17408\n"
"v_cvt_pk_bf16_f32 v11, v14, v15\n"
"v_exp_f32 v26, v26\n"
"v_add_f32 v237, v237, v24\n"
"v_exp_f32 v27, v27\n"
"v_mfma_f32_32x32x16_bf16 a[96:111], a[188:191], v[104:107], a[96:111]\n"
"ds_read_b128 a[152:155], v231 offset:17408\n"
"v_add_f32 v238, v238, v25\n"
"v_cvt_pk_bf16_f32 v24, v24, v25\n"
"v_exp_f32 v28, v28\n"
"v_add_f32 v237, v237, v26\n"
"v_mfma_f32_32x32x16_bf16 a[112:127], a[188:191], v[120:123], a[112:127]\n"
"s_add_u32 m0, s45, 2048\n"
"s_add_u32 s56, s49, 49152\n"
"s_and_b32 s56, s56, 0xfffff\n"
"buffer_load_dwordx4 v227, s[24:27], s56 offen lds\n"
"s_waitcnt lgkmcnt(3)\n"
"v_mfma_f32_32x32x16_bf16 v[64:79], a[128:131], v[160:163], v[128:143]\n"
"ds_read_b128 a[156:159], v232 offset:17408\n"
"buffer_load_dwordx4 v252, s[24:27], s56 offen offset:1024 lds\n"
"v_exp_f32 v29, v29\n"
"v_mfma_f32_32x32x16_bf16 v[80:95], a[128:131], v[192:195], v[144:159]\n"
"v_add_f32 v238, v238, v27\n"
"v_cvt_pk_bf16_f32 v25, v26, v27\n"
"v_exp_f32 v30, v30\n"
"v_add_f32 v237, v237, v28\n"
"v_exp_f32 v31, v31\n"
"v_mfma_f32_32x32x16_bf16 v[64:79], a[132:135], v[164:167], v[64:79]\n"
"ds_read_b128 a[160:163], v229 offset:24576\n"
"v_add_f32 v238, v238, v29\n"
"v_cvt_pk_bf16_f32 v26, v28, v29\n"
"v_exp_f32 v32, v32\n"
"v_add_f32 v237, v237, v30\n"
"v_mfma_f32_32x32x16_bf16 v[80:95], a[132:135], v[196:199], v[80:95]\n"
"v_exp_f32 v33, v33\n"
"v_add_f32 v238, v238, v31\n"
"v_cvt_pk_bf16_f32 v27, v30, v31\n"
"v_exp_f32 v34, v34\n"
"v_add_f32 v235, v235, v32\n"
"v_mfma_f32_32x32x16_bf16 v[64:79], a[136:139], v[168:171], v[64:79]\n"
"ds_read_b128 a[164:167], v230 offset:24576\n"
"s_cmp_lg_u32 s58, 0\n"
"s_cbranch_scc1 Lstub25_%=\n"
"Lret25_%=:\n"
"v_exp_f32 v35, v35\n"
"v_add_f32 v236, v236, v33\n"
"v_cvt_pk_bf16_f32 v32, v32, v33\n"
"v_mfma_f32_32x32x16_bf16 v[80:95], a[136:139], v[200:203], v[80:95]\n"
"s_add_u32 m0, s45, 114688\n"
"s_add_u32 s56, s49, 65536\n"
"s_and_b32 s56, s56, 0xfffff\n"
"buffer_load_dwordx4 v227, s[24:27], s56 offen lds\n"
"v_mfma_f32_32x32x16_bf16 v[64:79], a[140:143], v[172:175], v[64:79]\n"
"ds_read_b128 a[168:171], v231 offset:24576\n"
"buffer_load_dwordx4 v252, s[24:27], s56 offen offset:1024 lds\n"
"v_exp_f32 v36, v36\n"
"v_mfma_f32_32x32x16_bf16 v[80:95], a[140:143], v[204:207], v[80:95]\n"
"v_add_f32 v235, v235, v34\n"
"v_exp_f32 v37, v37\n"
"v_add_f32 v236, v236, v35\n"
"v_cvt_pk_bf16_f32 v33, v34, v35\n"
"v_exp_f32 v38, v38\n"
"s_waitcnt lgkmcnt(2)\n"
"v_mfma_f32_32x32x16_bf16 v[64:79], a[144:147], v[176:179], v[64:79]\n"
"ds_read_b128 a[172:175], v232 offset:24576\n"
"v_add_f32 v235, v235, v36\n"
"v_exp_f32 v39, v39\n"
"v_add_f32 v236, v236, v37\n"
"v_cvt_pk_bf16_f32 v34, v36, v37\n"
"v_mfma_f32_32x32x16_bf16 v[80:95], a[144:147], v[208:211], v[80:95]\n"
"v_exp_f32 v48, v48\n"
"v_add_f32 v235, v235, v38\n"
"v_exp_f32 v49, v49\n"
"v_add_f32 v236, v236, v39\n"
"v_cvt_pk_bf16_f32 v35, v38, v39\n"
"v_mfma_f32_32x32x16_bf16 v[64:79], a[148:151], v[180:183], v[64:79]\n"
"ds_read_b128 a[176:179], v229 offset:25600\n"
"v_exp_f32 v50, v50\n"
"v_add_f32 v237, v237, v48\n"
"v_exp_f32 v51, v51\n"
"v_add_f32 v238, v238, v49\n"
"v_mfma_f32_32x32x16_bf16 v[80:95], a[148:151], v[212:215], v[80:95]\n"
"s_add_u32 m0, s45, 116736\n"
"s_add_u32 s56, s49, 81920\n"
"s_and_b32 s56, s56, 0xfffff\n"
"buffer_load_dwordx4 v227, s[24:27], s56 offen lds\n"
"v_mfma_f32_32x32x16_bf16 v[64:79], a[152:155], v[184:187], v[64:79]\n"
"ds_read_b128 a[180:183], v230 offset:25600\n"
"buffer_load_dwordx4 v252, s[24:27], s56 offen offset:1024 lds\n"
"v_cvt_pk_bf16_f32 v48, v48, v49\n"
"v_mfma_f32_32x32x16_bf16 v[80:95], a[152:155], v[216:219], v[80:95]\n"
"v_exp_f32 v52, v52\n"
"v_add_f32 v237, v237, v50\n"
"v_exp_f32 v53, v53\n"
"v_add_f32 v238, v238, v51\n"
"v_cvt_pk_bf16_f32 v49, v50, v51\n"
"v_mfma_f32_32x32x16_bf16 v[64:79], a[156:159], v[188:191], v[64:79]\n"
"ds_read_b128 a[184:187], v231 offset:25600\n"
"v_exp_f32 v54, v54\n"
"v_add_f32 v237, v237, v52\n"
"v_exp_f32 v55, v55\n"
"v_add_f32 v238, v238, v53\n"
"v_mfma_f32_32x32x16_bf16 v[80:95], a[156:159], v[220:223], v[80:95]\n"
"v_cvt_pk_bf16_f32 v50, v52, v53\n"
"v_exp_f32 v40, v40\n"
"v_add_f32 v237, v237, v54\n"
"v_exp_f32 v41, v41\n"
"v_add_f32 v238, v238, v55\n"
"v_mfma_f32_32x32x16_bf16 v[96:111], a[160:163], v[160:163], v[128:143]\n"
"ds_read_b128 a[188:191], v232 offset:25600\n"
"v_cvt_pk_bf16_f32 v51, v54, v55\n"
"v_exp_f32 v42, v42\n"
"v_add_f32 v235, v235, v40\n"
"v_exp_f32 v43, v43\n"
"v_mfma_f32_32x32x16_bf16 v[112:127], a[160:163], v[192:195], v[144:159]\n"
"v_add_f32 v236, v236, v41\n"
"v_cvt_pk_bf16_f32 v40, v40, v41\n"
"v_exp_f32 v44, v44\n"
"v_add_f32 v235, v235, v42\n"
"v_exp_f32 v45, v45\n"
"s_waitcnt lgkmcnt(2)\n"
"v_mfma_f32_32x32x16_bf16 v[96:111], a[164:167], v[164:167], v[96:111]\n"
"ds_read_b64_tr_b16 a[128:129], v253 offset:0\n"
"v_add_f32 v236, v236, v43\n"
"v_cvt_pk_bf16_f32 v41, v42, v43\n"
"v_exp_f32 v46, v46\n"
"v_add_f32 v235, v235, v44\n"
"v_mfma_f32_32x32x16_bf16 v[112:127], a[164:167], v[196:199], v[112:127]\n"
"v_max_f32 v239, v64, v65\n"
"v_max_f32 v240, v80, v81\n"
"ds_read_b64_tr_b16 a[130:131], v253 offset:2048\n"
"v_exp_f32 v47, v47\n"
"v_add_f32 v236, v236, v45\n"
"v_cvt_pk_bf16_f32 v42, v44, v45\n"
"v_mfma_f32_32x32x16_bf16 v[96:111], a[168:171], v[168:171], v[96:111]\n"
"v_max3_f32 v239, v239, v66, v67\n"
"ds_read_b64_tr_b16 a[132:133], v254 offset:0\n"
"s_waitcnt vmcnt(10)\n"
"ds_read_b64_tr_b16 a[192:193], v226 offset:32768\n"
"ds_read_b64_tr_b16 a[194:195], v226 offset:33792\n"
"v_exp_f32 v56, v56\n"
"v_mfma_f32_32x32x16_bf16 v[112:127], a[168:171], v[200:203], v[112:127]\n"
"v_max3_f32 v240, v240, v82, v83\n"
"ds_read_b64_tr_b16 a[134:135], v254 offset:2048\n"
"ds_read_b64_tr_b16 a[196:197], v250 offset:32768\n"
"ds_read_b64_tr_b16 a[198:199], v250 offset:33792\n"
"v_add_f32 v235, v235, v46\n"
"v_exp_f32 v57, v57\n"
"v_mfma_f32_32x32x16_bf16 v[96:111], a[172:175], v[172:175], v[96:111]\n"
"v_max3_f32 v239, v239, v68, v69\n"
"ds_read_b64_tr_b16 a[136:137], v253 offset:1024\n"
"v_add_f32 v236, v236, v47\n"
"v_cvt_pk_bf16_f32 v43, v46, v47\n"
"v_exp_f32 v58, v58\n"
"v_add_f32 v237, v237, v56\n"
"v_mfma_f32_32x32x16_bf16 v[112:127], a[172:175], v[204:207], v[112:127]\n"
"v_max3_f32 v240, v240, v84, v85\n"
"v_max3_f32 v239, v239, v70, v71\n"
"ds_read_b64_tr_b16 a[138:139], v253 offset:3072\n"
"v_exp_f32 v59, v59\n"
"v_add_f32 v238, v238, v57\n"
"v_cvt_pk_bf16_f32 v56, v56, v57\n"
"v_mfma_f32_32x32x16_bf16 v[96:111], a[176:179], v[176:179], v[96:111]\n"
"v_max3_f32 v240, v240, v86, v87\n"
"ds_read_b64_tr_b16 a[140:141], v254 offset:1024\n"
"s_waitcnt lgkmcnt(3)\n"
"s_add_u32 m0, s45, 98304\n"
"s_add_u32 s56, s49, 294912\n"
"s_and_b32 s56, s56, 0xfffff\n"
"buffer_load_dwordx4 v227, s[24:27], s56 offen lds\n"
"s_waitcnt vmcnt(9)\n"
"ds_read_b64_tr_b16 a[200:201], v226 offset:34816\n"
"ds_read_b64_tr_b16 a[202:203], v226 offset:35840\n"
"v_mfma_f32_32x32x16_bf16 v[112:127], a[176:179], v[208:211], v[112:127]\n"
"v_max3_f32 v239, v239, v72, v73\n"
"ds_read_b64_tr_b16 a[142:143], v254 offset:3072\n"
"buffer_load_dwordx4 v252, s[24:27], s56 offen offset:1024 lds\n"
"ds_read_b64_tr_b16 a[204:205], v250 offset:34816\n"
"ds_read_b64_tr_b16 a[206:207], v250 offset:35840\n"
"v_mfma_f32_32x32x16_bf16 v[96:111], a[180:183], v[180:183], v[96:111]\n"
"v_max3_f32 v240, v240, v88, v89\n"
"ds_read_b64_tr_b16 a[144:145], v253 offset:4096\n"
"v_exp_f32 v60, v60\n"
"v_add_f32 v237, v237, v58\n"
"v_exp_f32 v61, v61\n"
"v_mfma_f32_32x32x16_bf16 v[112:127], a[180:183], v[212:215], v[112:127]\n"
"v_max3_f32 v239, v239, v74, v75\n"
"v_max3_f32 v240, v240, v90, v91\n"
"ds_read_b64_tr_b16 a[146:147], v253 offset:6144\n"
"v_add_f32 v238, v238, v59\n"
"v_cvt_pk_bf16_f32 v57, v58, v59\n"
"v_exp_f32 v62, v62\n"
"v_mfma_f32_32x32x16_bf16 v[96:111], a[184:187], v[184:187], v[96:111]\n"
"v_max3_f32 v239, v239, v76, v77\n"
"ds_read_b64_tr_b16 a[148:149], v254 offset:4096\n"
"s_waitcnt lgkmcnt(3)\n"
"s_add_u32 m0, s45, 100352\n"
"s_add_u32 s56, s49, 311296\n"
"s_and_b32 s56, s56, 0xfffff\n"
"buffer_load_dwordx4 v227, s[24:27], s56 offen lds\n"
"v_mfma_f32_32x32x16_bf16 v[112:127], a[184:187], v[216:219], v[112:127]\n"
"v_max3_f32 v240, v240, v92, v93\n"
"ds_read_b64_tr_b16 a[150:151], v254 offset:6144\n"
"buffer_load_dwordx4 v252, s[24:27], s56 offen offset:1024 lds\n"
"v_add_f32 v237, v237, v60\n"
"v_mfma_f32_32x32x16_bf16 v[96:111], a[188:191], v[188:191], v[96:111]\n"
"v_max3_f32 v239, v239, v78, v79\n"
"ds_read_b64_tr_b16 a[152:153], v253 offset:5120\n"
"v_exp_f32 v63, v63\n"
"v_add_f32 v238, v238, v61\n"
"v_cvt_pk_bf16_f32 v58, v60, v61\n"
"v_add_f32 v237, v237, v62\n"
"v_mfma_f32_32x32x16_bf16 v[112:127], a[188:191], v[220:223], v[112:127]\n"
"v_max3_f32 v240, v240, v94, v95\n"
"ds_read_b64_tr_b16 a[154:155], v253 offset:7168\n"
"v_add_f32 v238, v238, v63\n"
"v_cvt_pk_bf16_f32 v59, v62, v63\n"
"v_mfma_f32_32x32x16_bf16 a[0:15], a[128:131], v[0:3], a[0:15]\n"
"ds_read_b64_tr_b16 a[156:157], v254 offset:5120\n"
"v_mfma_f32_32x32x16_bf16 a[16:31], a[128:131], v[16:19], a[16:31]\n"
"ds_read_b64_tr_b16 a[158:159], v254 offset:7168\n"
"v_mfma_f32_32x32x16_bf16 a[32:47], a[132:135], v[0:3], a[32:47]\n"
"ds_read_b64_tr_b16 a[160:161], v253 offset:8192\n"
"s_waitcnt vmcnt(10)\n"
"ds_read_b64_tr_b16 a[208:209], v224 offset:0\n"
"ds_read_b64_tr_b16 a[210:211], v224 offset:1024\n"
"v_mfma_f32_32x32x16_bf16 a[48:63], a[132:135], v[16:19], a[48:63]\n"
"ds_read_b64_tr_b16 a[162:163], v253 offset:10240\n"
"v_max3_f32 v239, v239, v96, v97\n"
"v_max3_f32 v240, v240, v112, v113\n"
"ds_read_b64_tr_b16 a[212:213], v225 offset:0\n"
"ds_read_b64_tr_b16 a[214:215], v225 offset:1024\n"
"v_mfma_f32_32x32x16_bf16 a[64:79], a[136:139], v[0:3], a[64:79]\n"
"ds_read_b64_tr_b16 a[164:165], v254 offset:8192\n"
"v_max3_f32 v239, v239, v98, v99\n"
"v_max3_f32 v240, v240, v114, v115\n"
"v_mfma_f32_32x32x16_bf16 a[80:95], a[136:139], v[16:19], a[80:95]\n"
"ds_read_b64_tr_b16 a[166:167], v254 offset:10240\n"
"v_max3_f32 v239, v239, v100, v101\n"
"v_max3_f32 v240, v240, v116, v117\n"
"v_mfma_f32_32x32x16_bf16 a[96:111], a[140:143], v[0:3], a[96:111]\n"
"ds_read_b64_tr_b16 a[168:169], v253 offset:9216\n"
"v_max3_f32 v239, v239, v102, v103\n"
"v_max3_f32 v240, v240, v118, v119\n"
"s_waitcnt lgkmcnt(3)\n"
"s_add_u32 m0, s45, 0\n"
"s_add_u32 s56, s49, 327680\n"
"s_and_b32 s56, s56, 0xfffff\n"
"buffer_load_dwordx4 v227, s[24:27], s56 offen lds\n"
"v_mfma_f32_32x32x16_bf16 a[112:127], a[140:143], v[16:19], a[112:127]\n"
"ds_read_b64_tr_b16 a[170:171], v253 offset:11264\n"
"v_max3_f32 v239, v239, v104, v105\n"
"buffer_load_dwordx4 v252, s[24:27], s56 offen offset:1024 lds\n"
"v_mfma_f32_32x32x16_bf16 a[0:15], a[144:147], v[8:11], a[0:15]\n"
"ds_read_b64_tr_b16 a[172:173], v254 offset:9216\n"
"v_max3_f32 v240, v240, v120, v121\n"
"v_max3_f32 v239, v239, v106, v107\n"
"s_waitcnt vmcnt(10)\n"
"ds_read_b64_tr_b16 a[216:217], v224 offset:2048\n"
"ds_read_b64_tr_b16 a[218:219], v224 offset:3072\n"
"v_mfma_f32_32x32x16_bf16 a[16:31], a[144:147], v[24:27], a[16:31]\n"
"v_max3_f32 v240, v240, v122, v123\n"
"v_max3_f32 v239, v239, v108, v109\n"
"ds_read_b64_tr_b16 a[220:221], v225 offset:2048\n"
"ds_read_b64_tr_b16 a[222:223], v225 offset:3072\n"
"v_mfma_f32_32x32x16_bf16 a[32:47], a[148:151], v[8:11], a[32:47]\n"
"ds_read_b64_tr_b16 a[174:175], v254 offset:11264\n"
"v_max3_f32 v240, v240, v124, v125\n"
"v_max3_f32 v239, v239, v110, v111\n"
"v_mfma_f32_32x32x16_bf16 a[48:63], a[148:151], v[24:27], a[48:63]\n"
"ds_read_b64_tr_b16 a[176:177], v253 offset:12288\n"
"v_max3_f32 v240, v240, v126, v127\n"
"v_mfma_f32_32x32x16_bf16 a[64:79], a[152:155], v[8:11], a[64:79]\n"
"ds_read_b64_tr_b16 a[178:179], v253 offset:14336\n"
"v_max_f32 v239, v239, v240\n"
"v_cmp_lt_f32 vcc, 0x41000000, v239\n"
"s_nop 0\n"
"s_cbranch_vccnz Lstub5_%=\n"
"Lret5_%=:\n"
"s_waitcnt lgkmcnt(3)\n"
"s_add_u32 m0, s45, 2048\n"
"s_add_u32 s56, s49, 344064\n"
"s_and_b32 s56, s56, 0xfffff\n"
"buffer_load_dwordx4 v227, s[24:27], s56 offen lds\n"
"v_mfma_f32_32x32x16_bf16 a[80:95], a[152:155], v[24:27], a[80:95]\n"
"ds_read_b64_tr_b16 a[180:181], v254 offset:12288\n"
"buffer_load_dwordx4 v252, s[24:27], s56 offen offset:1024 lds\n"
"v_exp_f32 v64, v64\n"
"v_mfma_f32_32x32x16_bf16 a[96:111], a[156:159], v[8:11], a[96:111]\n"
"ds_read_b64_tr_b16 a[182:183], v254 offset:14336\n"
"s_waitcnt vmcnt(10)\n"
"ds_read_b64_tr_b16 a[224:225], v226 offset:49152\n"
"ds_read_b64_tr_b16 a[226:227], v226 offset:50176\n"
"v_exp_f32 v65, v65\n"
"v_mfma_f32_32x32x16_bf16 a[112:127], a[156:159], v[24:27], a[112:127]\n"
"ds_read_b64_tr_b16 a[184:185], v253 offset:13312\n"
"ds_read_b64_tr_b16 a[228:229], v250 offset:49152\n"
"ds_read_b64_tr_b16 a[230:231], v250 offset:50176\n"
"v_exp_f32 v66, v66\n"
"v_add_f32 v235, v235, v64\n"
"v_mfma_f32_32x32x16_bf16 a[0:15], a[160:163], v[32:35], a[0:15]\n"
"ds_read_b64_tr_b16 a[186:187], v253 offset:15360\n"
"v_exp_f32 v67, v67\n"
"v_add_f32 v236, v236, v65\n"
"v_cvt_pk_bf16_f32 v64, v64, v65\n"
"v_exp_f32 v68, v68\n"
"v_mfma_f32_32x32x16_bf16 a[16:31], a[160:163], v[48:51], a[16:31]\n"
"ds_read_b64_tr_b16 a[188:189], v254 offset:13312\n"
"v_add_f32 v235, v235, v66\n"
"v_exp_f32 v69, v69\n"
"v_add_f32 v236, v236, v67\n"
"v_cvt_pk_bf16_f32 v65, v66, v67\n"
"v_mfma_f32_32x32x16_bf16 a[32:47], a[164:167], v[32:35], a[32:47]\n"
"ds_read_b64_tr_b16 a[190:191], v254 offset:15360\n"
"s_waitcnt lgkmcnt(3)\n"
"s_add_u32 m0, s45, 114688\n"
"s_add_u32 s56, s49, 360448\n"
"s_and_b32 s56, s56, 0xfffff\n"
"buffer_load_dwordx4 v227, s[24:27], s56 offen lds\n"
"v_mfma_f32_32x32x16_bf16 a[48:63], a[164:167], v[48:51], a[48:63]\n"
"buffer_load_dwordx4 v252, s[24:27], s56 offen offset:1024 lds\n"
"v_exp_f32 v70, v70\n"
"v_add_f32 v235, v235, v68\n"
"v_mfma_f32_32x32x16_bf16 a[64:79], a[168:171], v[32:35], a[64:79]\n"
"s_waitcnt vmcnt(10)\n"
"ds_read_b64_tr_b16 a[232:233], v226 offset:51200\n"
"ds_read_b64_tr_b16 a[234:235], v226 offset:52224\n"
"v_exp_f32 v71, v71\n"
"v_add_f32 v236, v236, v69\n"
"v_cvt_pk_bf16_f32 v66, v68, v69\n"
"v_mfma_f32_32x32x16_bf16 a[80:95], a[168:171], v[48:51], a[80:95]\n"
"ds_read_b64_tr_b16 a[236:237], v250 offset:51200\n"
"ds_read_b64_tr_b16 a[238:239], v250 offset:52224\n"
"v_exp_f32 v80, v80\n"
"v_add_f32 v235, v235, v70\n"
"v_exp_f32 v81, v81\n"
"v_mfma_f32_32x32x16_bf16 a[96:111], a[172:175], v[32:35], a[96:111]\n"
"v_add_f32 v236, v236, v71\n"
"v_cvt_pk_bf16_f32 v67, v70, v71\n"
"v_exp_f32 v82, v82\n"
"v_add_f32 v237, v237, v80\n"
"v_exp_f32 v83, v83\n"
"v_mfma_f32_32x32x16_bf16 a[112:127], a[172:175], v[48:51], a[112:127]\n"
"v_add_f32 v238, v238, v81\n"
"v_cvt_pk_bf16_f32 v80, v80, v81\n"
"v_exp_f32 v84, v84\n"
"v_add_f32 v237, v237, v82\n"
"v_exp_f32 v85, v85\n"
"v_mfma_f32_32x32x16_bf16 a[0:15], a[176:179], v[40:43], a[0:15]\n"
"s_waitcnt lgkmcnt(0)\n"
"s_barrier\n"
"ds_read_b128 a[128:131], v229 offset:32768\n"
"v_add_f32 v238, v238, v83\n"
"v_cvt_pk_bf16_f32 v81, v82, v83\n"
"v_exp_f32 v86, v86\n"
"v_add_f32 v237, v237, v84\n"
"v_mfma_f32_32x32x16_bf16 a[16:31], a[176:179], v[56:59], a[16:31]\n"
"ds_read_b128 a[132:135], v230 offset:32768\n"
"s_add_u32 m0, s45, 16384\n"
"s_add_u32 s56, s49, 98304\n"
"s_and_b32 s56, s56, 0xfffff\n"
"buffer_load_dwordx4 v227, s[24:27], s56 offen lds\n"
"v_mfma_f32_32x32x16_bf16 a[32:47], a[180:183], v[40:43], a[32:47]\n"
"ds_read_b128 a[136:139], v231 offset:32768\n"
"buffer_load_dwordx4 v252, s[24:27], s56 offen offset:1024 lds\n"
"v_exp_f32 v87, v87\n"
"v_mfma_f32_32x32x16_bf16 a[48:63], a[180:183], v[56:59], a[48:63]\n"
"ds_read_b128 a[140:143], v232 offset:32768\n"
"s_add_u32 m0, s45, 116736\n"
"s_add_u32 s56, s49, 376832\n"
"s_and_b32 s56, s56, 0xfffff\n"
"buffer_load_dwordx4 v227, s[24:27], s56 offen lds\n"
"v_mfma_f32_32x32x16_bf16 a[64:79], a[184:187], v[40:43], a[64:79]\n"
"ds_read_b128 a[144:147], v229 offset:33792\n"
"buffer_load_dwordx4 v252, s[24:27], s56 offen offset:1024 lds\n"
"v_add_f32 v238, v238, v85\n"
"v_cvt_pk_bf16_f32 v82, v84, v85\n"
"v_mfma_f32_32x32x16_bf16 a[80:95], a[184:187], v[56:59], a[80:95]\n"
"ds_read_b128 a[148:151], v230 offset:33792\n"
"v_exp_f32 v72, v72\n"
"v_add_f32 v237, v237, v86\n"
"v_exp_f32 v73, v73\n"
"v_add_f32 v238, v238, v87\n"
"v_mfma_f32_32x32x16_bf16 a[96:111], a[188:191], v[40:43], a[96:111]\n"
"ds_read_b128 a[152:155], v231 offset:33792\n"
"v_cvt_pk_bf16_f32 v83, v86, v87\n"
"v_exp_f32 v74, v74\n"
"v_add_f32 v235, v235, v72\n"
"v_exp_f32 v75, v75\n"
"v_mfma_f32_32x32x16_bf16 a[112:127], a[188:191], v[56:59], a[112:127]\n"
"s_add_u32 m0, s45, 18432\n"
"s_add_u32 s56, s49, 114688\n"
"s_and_b32 s56, s56, 0xfffff\n"
"buffer_load_dwordx4 v227, s[24:27], s56 offen lds\n"
"s_waitcnt lgkmcnt(3)\n"
"v_mfma_f32_32x32x16_bf16 v[0:15], a[128:131], v[160:163], v[128:143]\n"
"ds_read_b128 a[156:159], v232 offset:33792\n"
"buffer_load_dwordx4 v252, s[24:27], s56 offen offset:1024 lds\n"
"v_add_f32 v236, v236, v73\n"
"v_cvt_pk_bf16_f32 v72, v72, v73\n"
"v_mfma_f32_32x32x16_bf16 v[16:31], a[128:131], v[192:195], v[144:159]\n"
"s_waitcnt vmcnt(14)\n"
"ds_read_b64_tr_b16 a[240:241], v226 offset:32768\n"
"ds_read_b64_tr_b16 a[242:243], v226 offset:33792\n"
"v_exp_f32 v76, v76\n"
"v_add_f32 v235, v235, v74\n"
"v_mfma_f32_32x32x16_bf16 v[0:15], a[132:135], v[164:167], v[0:15]\n"
"ds_read_b128 a[160:163], v229 offset:40960\n"
"ds_read_b64_tr_b16 a[244:245], v250 offset:32768\n"
"ds_read_b64_tr_b16 a[246:247], v250 offset:33792\n"
"v_exp_f32 v77, v77\n"
"v_add_f32 v236, v236, v75\n"
"v_cvt_pk_bf16_f32 v73, v74, v75\n"
"v_mfma_f32_32x32x16_bf16 v[16:31], a[132:135], v[196:199], v[16:31]\n"
"v_exp_f32 v78, v78\n"
"v_add_f32 v235, v235, v76\n"
"v_exp_f32 v79, v79\n"
"v_add_f32 v236, v236, v77\n"
"v_cvt_pk_bf16_f32 v74, v76, v77\n"
"v_mfma_f32_32x32x16_bf16 v[0:15], a[136:139], v[168:171], v[0:15]\n"
"ds_read_b128 a[164:167], v230 offset:40960\n"
"s_cmp_lg_u32 s58, 0\n"
"s_cbranch_scc1 Lstub26_%=\n"
"Lret26_%=:\n"
"v_exp_f32 v88, v88\n"
"v_add_f32 v235, v235, v78\n"
"v_mfma_f32_32x32x16_bf16 v[16:31], a[136:139], v[200:203], v[16:31]\n"
"s_add_u32 m0, s45, 131072\n"
"s_add_u32 s56, s49, 131072\n"
"s_and_b32 s56, s56, 0xfffff\n"
"buffer_load_dwordx4 v227, s[24:27], s56 offen lds\n"
"s_waitcnt vmcnt(13)\n"
"ds_read_b64_tr_b16 a[248:249], v226 offset:34816\n"
"ds_read_b64_tr_b16 a[250:251], v226 offset:35840\n"
"v_mfma_f32_32x32x16_bf16 v[0:15], a[140:143], v[172:175], v[0:15]\n"
"ds_read_b128 a[168:171], v231 offset:40960\n"
"buffer_load_dwordx4 v252, s[24:27], s56 offen offset:1024 lds\n"
"ds_read_b64_tr_b16 a[252:253], v250 offset:34816\n"
"ds_read_b64_tr_b16 a[254:255], v250 offset:35840\n"
"v_mfma_f32_32x32x16_bf16 v[16:31], a[140:143], v[204:207], v[16:31]\n"
"s_waitcnt lgkmcnt(6)\n"
"s_add_u32 m0, s45, 98304\n"
"s_add_u32 s56, s49, 393216\n"
"s_and_b32 s56, s56, 0xfffff\n"
"buffer_load_dwordx4 v227, s[24:27], s56 offen lds\n"
"v_mfma_f32_32x32x16_bf16 v[0:15], a[144:147], v[176:179], v[0:15]\n"
"ds_read_b128 a[172:175], v232 offset:40960\n"
"buffer_load_dwordx4 v252, s[24:27], s56 offen offset:1024 lds\n"
"v_exp_f32 v89, v89\n"
"v_mfma_f32_32x32x16_bf16 v[16:31], a[144:147], v[208:211], v[16:31]\n"
"s_waitcnt lgkmcnt(1)\n"
"s_add_u32 m0, s45, 100352\n"
"s_add_u32 s56, s49, 409600\n"
"s_and_b32 s56, s56, 0xfffff\n"
"buffer_load_dwordx4 v227, s[24:27], s56 offen lds\n"
"v_mfma_f32_32x32x16_bf16 v[0:15], a[148:151], v[180:183], v[0:15]\n"
"ds_read_b128 a[176:179], v229 offset:41984\n"
"buffer_load_dwordx4 v252, s[24:27], s56 offen offset:1024 lds\n"
"v_add_f32 v236, v236, v79\n"
"v_cvt_pk_bf16_f32 v75, v78, v79\n"
"v_mfma_f32_32x32x16_bf16 v[16:31], a[148:151], v[212:215], v[16:31]\n"
"s_add_u32 m0, s45, 133120\n"
"s_add_u32 s56, s49, 147456\n"
"s_and_b32 s56, s56, 0xfffff\n"
"buffer_load_dwordx4 v227, s[24:27], s56 offen lds\n"
"v_mfma_f32_32x32x16_bf16 v[0:15], a[152:155], v[184:187], v[0:15]\n"
"ds_read_b128 a[180:183], v230 offset:41984\n"
"buffer_load_dwordx4 v252, s[24:27], s56 offen offset:1024 lds\n"
"v_exp_f32 v90, v90\n"
"v_mfma_f32_32x32x16_bf16 v[16:31], a[152:155], v[216:219], v[16:31]\n"
"v_add_f32 v237, v237, v88\n"
"v_exp_f32 v91, v91\n"
"v_add_f32 v238, v238, v89\n"
"v_cvt_pk_bf16_f32 v88, v88, v89\n"
"v_exp_f32 v92, v92\n"
"v_mfma_f32_32x32x16_bf16 v[0:15], a[156:159], v[188:191], v[0:15]\n"
"ds_read_b128 a[184:187], v231 offset:41984\n"
"v_add_f32 v237, v237, v90\n"
"v_exp_f32 v93, v93\n"
"v_add_f32 v238, v238, v91\n"
"v_cvt_pk_bf16_f32 v89, v90, v91\n"
"v_mfma_f32_32x32x16_bf16 v[16:31], a[156:159], v[220:223], v[16:31]\n"
"v_exp_f32 v94, v94\n"
"v_add_f32 v237, v237, v92\n"
"v_exp_f32 v95, v95\n"
"v_add_f32 v238, v238, v93\n"
"v_cvt_pk_bf16_f32 v90, v92, v93\n"
"v_mfma_f32_32x32x16_bf16 v[32:47], a[160:163], v[160:163], v[128:143]\n"
"ds_read_b128 a[188:191], v232 offset:41984\n"
"v_exp_f32 v96, v96\n"
"v_add_f32 v237, v237, v94\n"
"v_exp_f32 v97, v97\n"
"v_add_f32 v238, v238, v95\n"
"v_mfma_f32_32x32x16_bf16 v[48:63], a[160:163], v[192:195], v[144:159]\n"
"v_cvt_pk_bf16_f32 v91, v94, v95\n"
"v_exp_f32 v98, v98\n"
"v_add_f32 v235, v235, v96\n"
"v_exp_f32 v99, v99\n"
"v_add_f32 v236, v236, v97\n"
"v_mfma_f32_32x32x16_bf16 v[32:47], a[164:167], v[164:167], v[32:47]\n"
"ds_read_b64_tr_b16 a[128:129], v253 offset:16384\n"
"v_cvt_pk_bf16_f32 v96, v96, v97\n"
"v_exp_f32 v100, v100\n"
"v_add_f32 v235, v235, v98\n"
"v_exp_f32 v101, v101\n"
"v_mfma_f32_32x32x16_bf16 v[48:63], a[164:167], v[196:199], v[48:63]\n"
"v_max_f32 v239, v0, v1\n"
"v_max_f32 v240, v16, v17\n"
"ds_read_b64_tr_b16 a[130:131], v253 offset:18432\n"
"v_add_f32 v236, v236, v99\n"
"v_cvt_pk_bf16_f32 v97, v98, v99\n"
"v_exp_f32 v102, v102\n"
"v_mfma_f32_32x32x16_bf16 v[32:47], a[168:171], v[168:171], v[32:47]\n"
"v_max3_f32 v239, v239, v2, v3\n"
"ds_read_b64_tr_b16 a[132:133], v254 offset:16384\n"
"v_add_f32 v235, v235, v100\n"
"v_exp_f32 v103, v103\n"
"v_add_f32 v236, v236, v101\n"
"v_cvt_pk_bf16_f32 v98, v100, v101\n"
"v_mfma_f32_32x32x16_bf16 v[48:63], a[168:171], v[200:203], v[48:63]\n"
"v_max3_f32 v240, v240, v18, v19\n"
"ds_read_b64_tr_b16 a[134:135], v254 offset:18432\n"
"v_exp_f32 v112, v112\n"
"v_add_f32 v235, v235, v102\n"
"v_exp_f32 v113, v113\n"
"s_waitcnt lgkmcnt(3)\n"
"v_mfma_f32_32x32x16_bf16 v[32:47], a[172:175], v[172:175], v[32:47]\n"
"v_max3_f32 v239, v239, v4, v5\n"
"ds_read_b64_tr_b16 a[136:137], v253 offset:17408\n"
"v_add_f32 v236, v236, v103\n"
"v_cvt_pk_bf16_f32 v99, v102, v103\n"
"v_exp_f32 v114, v114\n"
"v_add_f32 v237, v237, v112\n"
"v_mfma_f32_32x32x16_bf16 v[48:63], a[172:175], v[204:207], v[48:63]\n"
"v_max3_f32 v240, v240, v20, v21\n"
"v_max3_f32 v239, v239, v6, v7\n"
"ds_read_b64_tr_b16 a[138:139], v253 offset:19456\n"
"v_exp_f32 v115, v115\n"
"v_add_f32 v238, v238, v113\n"
"v_cvt_pk_bf16_f32 v112, v112, v113\n"
"v_mfma_f32_32x32x16_bf16 v[32:47], a[176:179], v[176:179], v[32:47]\n"
"v_max3_f32 v240, v240, v22, v23\n"
"ds_read_b64_tr_b16 a[140:141], v254 offset:17408\n"
"v_exp_f32 v116, v116\n"
"v_add_f32 v237, v237, v114\n"
"v_exp_f32 v117, v117\n"
"v_mfma_f32_32x32x16_bf16 v[48:63], a[176:179], v[208:211], v[48:63]\n"
"v_max3_f32 v239, v239, v8, v9\n"
"ds_read_b64_tr_b16 a[142:143], v254 offset:19456\n"
"v_add_f32 v238, v238, v115\n"
"v_cvt_pk_bf16_f32 v113, v114, v115\n"
"v_exp_f32 v118, v118\n"
"v_add_f32 v237, v237, v116\n"
"v_mfma_f32_32x32x16_bf16 v[32:47], a[180:183], v[180:183], v[32:47]\n"
"v_max3_f32 v240, v240, v24, v25\n"
"ds_read_b64_tr_b16 a[144:145], v253 offset:20480\n"
"v_exp_f32 v119, v119\n"
"v_add_f32 v238, v238, v117\n"
"v_cvt_pk_bf16_f32 v114, v116, v117\n"
"v_mfma_f32_32x32x16_bf16 v[48:63], a[180:183], v[212:215], v[48:63]\n"
"v_max3_f32 v239, v239, v10, v11\n"
"v_max3_f32 v240, v240, v26, v27\n"
"ds_read_b64_tr_b16 a[146:147], v253 offset:22528\n"
"v_exp_f32 v104, v104\n"
"v_add_f32 v237, v237, v118\n"
"v_mfma_f32_32x32x16_bf16 v[32:47], a[184:187], v[184:187], v[32:47]\n"
"v_max3_f32 v239, v239, v12, v13\n"
"ds_read_b64_tr_b16 a[148:149], v254 offset:20480\n"
"v_exp_f32 v105, v105\n"
"v_add_f32 v238, v238, v119\n"
"v_cvt_pk_bf16_f32 v115, v118, v119\n"
"v_mfma_f32_32x32x16_bf16 v[48:63], a[184:187], v[216:219], v[48:63]\n"
"v_max3_f32 v240, v240, v28, v29\n"
"ds_read_b64_tr_b16 a[150:151], v254 offset:22528\n"
"v_exp_f32 v106, v106\n"
"v_add_f32 v235, v235, v104\n"
"v_exp_f32 v107, v107\n"
"v_mfma_f32_32x32x16_bf16 v[32:47], a[188:191], v[188:191], v[32:47]\n"
"v_max3_f32 v239, v239, v14, v15\n"
"ds_read_b64_tr_b16 a[152:153], v253 offset:21504\n"
"v_add_f32 v236, v236, v105\n"
"v_cvt_pk_bf16_f32 v104, v104, v105\n"
"v_exp_f32 v108, v108\n"
"v_add_f32 v235, v235, v106\n"
"v_mfma_f32_32x32x16_bf16 v[48:63], a[188:191], v[220:223], v[48:63]\n"
"v_max3_f32 v240, v240, v30, v31\n"
"ds_read_b64_tr_b16 a[154:155], v253 offset:23552\n"
"v_exp_f32 v109, v109\n"
"v_add_f32 v236, v236, v107\n"
"v_cvt_pk_bf16_f32 v105, v106, v107\n"
"s_waitcnt lgkmcnt(4)\n"
"v_mfma_f32_32x32x16_bf16 a[0:15], a[128:131], v[64:67], a[0:15]\n"
"ds_read_b64_tr_b16 a[156:157], v254 offset:21504\n"
"v_exp_f32 v110, v110\n"
"v_add_f32 v235, v235, v108\n"
"v_exp_f32 v111, v111\n"
"v_add_f32 v236, v236, v109\n"
"v_mfma_f32_32x32x16_bf16 a[16:31], a[128:131], v[80:83], a[16:31]\n"
"ds_read_b64_tr_b16 a[158:159], v254 offset:23552\n"
"v_cvt_pk_bf16_f32 v106, v108, v109\n"
"v_exp_f32 v120, v120\n"
"v_add_f32 v235, v235, v110\n"
"v_exp_f32 v121, v121\n"
"v_mfma_f32_32x32x16_bf16 a[32:47], a[132:135], v[64:67], a[32:47]\n"
"ds_read_b64_tr_b16 a[160:161], v253 offset:24576\n"
"v_add_f32 v236, v236, v111\n"
"v_cvt_pk_bf16_f32 v107, v110, v111\n"
"v_exp_f32 v122, v122\n"
"v_add_f32 v237, v237, v120\n"
"v_mfma_f32_32x32x16_bf16 a[48:63], a[132:135], v[80:83], a[48:63]\n"
"ds_read_b64_tr_b16 a[162:163], v253 offset:26624\n"
"v_max3_f32 v239, v239, v32, v33\n"
"v_max3_f32 v240, v240, v48, v49\n"
"v_exp_f32 v123, v123\n"
"v_add_f32 v238, v238, v121\n"
"v_cvt_pk_bf16_f32 v120, v120, v121\n"
"v_mfma_f32_32x32x16_bf16 a[64:79], a[136:139], v[64:67], a[64:79]\n"
"ds_read_b64_tr_b16 a[164:165], v254 offset:24576\n"
"v_max3_f32 v239, v239, v34, v35\n"
"v_max3_f32 v240, v240, v50, v51\n"
"v_exp_f32 v124, v124\n"
"v_add_f32 v237, v237, v122\n"
"v_mfma_f32_32x32x16_bf16 a[80:95], a[136:139], v[80:83], a[80:95]\n"
"ds_read_b64_tr_b16 a[166:167], v254 offset:26624\n"
"v_max3_f32 v239, v239, v36, v37\n"
"v_max3_f32 v240, v240, v52, v53\n"
"v_exp_f32 v125, v125\n"
"v_add_f32 v238, v238, v123\n"
"v_cvt_pk_bf16_f32 v121, v122, v123\n"
"v_mfma_f32_32x32x16_bf16 a[96:111], a[140:143], v[64:67], a[96:111]\n"
"ds_read_b64_tr_b16 a[168:169], v253 offset:25600\n"
"v_max3_f32 v239, v239, v38, v39\n"
"v_max3_f32 v240, v240, v54, v55\n"
"v_exp_f32 v126, v126\n"
"v_add_f32 v237, v237, v124\n"
"v_mfma_f32_32x32x16_bf16 a[112:127], a[140:143], v[80:83], a[112:127]\n"
"ds_read_b64_tr_b16 a[170:171], v253 offset:27648\n"
"v_max3_f32 v239, v239, v40, v41\n"
"v_exp_f32 v127, v127\n"
"v_add_f32 v238, v238, v125\n"
"v_cvt_pk_bf16_f32 v122, v124, v125\n"
"v_add_f32 v237, v237, v126\n"
"v_mfma_f32_32x32x16_bf16 a[0:15], a[144:147], v[72:75], a[0:15]\n"
"ds_read_b64_tr_b16 a[172:173], v254 offset:25600\n"
"v_max3_f32 v240, v240, v56, v57\n"
"v_max3_f32 v239, v239, v42, v43\n"
"v_add_f32 v238, v238, v127\n"
"v_cvt_pk_bf16_f32 v123, v126, v127\n"
"v_mfma_f32_32x32x16_bf16 a[16:31], a[144:147], v[88:91], a[16:31]\n"
"v_max3_f32 v240, v240, v58, v59\n"
"v_max3_f32 v239, v239, v44, v45\n"
"s_waitcnt lgkmcnt(3)\n"
"v_mfma_f32_32x32x16_bf16 a[32:47], a[148:151], v[72:75], a[32:47]\n"
"ds_read_b64_tr_b16 a[174:175], v254 offset:27648\n"
"v_max3_f32 v240, v240, v60, v61\n"
"v_max3_f32 v239, v239, v46, v47\n"
"v_mfma_f32_32x32x16_bf16 a[48:63], a[148:151], v[88:91], a[48:63]\n"
"ds_read_b64_tr_b16 a[176:177], v253 offset:28672\n"
"v_max3_f32 v240, v240, v62, v63\n"
"v_mfma_f32_32x32x16_bf16 a[64:79], a[152:155], v[72:75], a[64:79]\n"
"ds_read_b64_tr_b16 a[178:179], v253 offset:30720\n"
"v_max_f32 v239, v239, v240\n"
"v_cmp_lt_f32 vcc, 0x41000000, v239\n"
"s_nop 0\n"
"s_cbranch_vccnz Lstub6_%=\n"
"Lret6_%=:\n"
"v_mfma_f32_32x32x16_bf16 a[80:95], a[152:155], v[88:91], a[80:95]\n"
"ds_read_b64_tr_b16 a[180:181], v254 offset:28672\n"
"v_exp_f32 v0, v0\n"
"v_exp_f32 v1, v1\n"
"v_mfma_f32_32x32x16_bf16 a[96:111], a[156:159], v[72:75], a[96:111]\n"
"ds_read_b64_tr_b16 a[182:183], v254 offset:30720\n"
"v_exp_f32 v2, v2\n"
"v_add_f32 v235, v235, v0\n"
"v_exp_f32 v3, v3\n"
"v_mfma_f32_32x32x16_bf16 a[112:127], a[156:159], v[88:91], a[112:127]\n"
"ds_read_b64_tr_b16 a[184:185], v253 offset:29696\n"
"v_add_f32 v236, v236, v1\n"
"v_cvt_pk_bf16_f32 v0, v0, v1\n"
"v_exp_f32 v4, v4\n"
"v_add_f32 v235, v235, v2\n"
"v_mfma_f32_32x32x16_bf16 a[0:15], a[160:163], v[96:99], a[0:15]\n"
"ds_read_b64_tr_b16 a[186:187], v253 offset:31744\n"
"v_exp_f32 v5, v5\n"
"v_add_f32 v236, v236, v3\n"
"v_cvt_pk_bf16_f32 v1, v2, v3\n"
"v_mfma_f32_32x32x16_bf16 a[16:31], a[160:163], v[112:115], a[16:31]\n"
"ds_read_b64_tr_b16 a[188:189], v254 offset:29696\n"
"v_exp_f32 v6, v6\n"
"v_add_f32 v235, v235, v4\n"
"v_exp_f32 v7, v7\n"
"v_mfma_f32_32x32x16_bf16 a[32:47], a[164:167], v[96:99], a[32:47]\n"
"ds_read_b64_tr_b16 a[190:191], v254 offset:31744\n"
"v_add_f32 v236, v236, v5\n"
"v_cvt_pk_bf16_f32 v2, v4, v5\n"
"v_exp_f32 v16, v16\n"
"v_add_f32 v235, v235, v6\n"
"v_mfma_f32_32x32x16_bf16 a[48:63], a[164:167], v[112:115], a[48:63]\n"
"v_exp_f32 v17, v17\n"
"v_add_f32 v236, v236, v7\n"
"v_cvt_pk_bf16_f32 v3, v6, v7\n"
"v_exp_f32 v18, v18\n"
"s_waitcnt lgkmcnt(3)\n"
"v_mfma_f32_32x32x16_bf16 a[64:79], a[168:171], v[96:99], a[64:79]\n"
"v_add_f32 v237, v237, v16\n"
"v_exp_f32 v19, v19\n"
"v_add_f32 v238, v238, v17\n"
"v_cvt_pk_bf16_f32 v16, v16, v17\n"
"v_mfma_f32_32x32x16_bf16 a[80:95], a[168:171], v[112:115], a[80:95]\n"
"v_exp_f32 v20, v20\n"
"v_add_f32 v237, v237, v18\n"
"v_exp_f32 v21, v21\n"
"v_add_f32 v238, v238, v19\n"
"v_mfma_f32_32x32x16_bf16 a[96:111], a[172:175], v[96:99], a[96:111]\n"
"v_cvt_pk_bf16_f32 v17, v18, v19\n"
"v_exp_f32 v22, v22\n"
"v_add_f32 v237, v237, v20\n"
"v_exp_f32 v23, v23\n"
"v_mfma_f32_32x32x16_bf16 a[112:127], a[172:175], v[112:115], a[112:127]\n"
"v_add_f32 v238, v238, v21\n"
"v_cvt_pk_bf16_f32 v18, v20, v21\n"
"v_exp_f32 v8, v8\n"
"v_add_f32 v237, v237, v22\n"
"v_mfma_f32_32x32x16_bf16 a[0:15], a[176:179], v[104:107], a[0:15]\n"
"s_waitcnt lgkmcnt(0)\n"
"s_barrier\n"
"ds_read_b128 a[128:131], v229 offset:49152\n"
"v_exp_f32 v9, v9\n"
"v_add_f32 v238, v238, v23\n"
"v_cvt_pk_bf16_f32 v19, v22, v23\n"
"v_mfma_f32_32x32x16_bf16 a[16:31], a[176:179], v[120:123], a[16:31]\n"
"ds_read_b128 a[132:135], v230 offset:49152\n"
"s_add_u32 m0, s45, 32768\n"
"s_add_u32 s56, s49, 163840\n"
"s_and_b32 s56, s56, 0xfffff\n"
"buffer_load_dwordx4 v227, s[24:27], s56 offen lds\n"
"v_mfma_f32_32x32x16_bf16 a[32:47], a[180:183], v[104:107], a[32:47]\n"
"ds_read_b128 a[136:139], v231 offset:49152\n"
"buffer_load_dwordx4 v252, s[24:27], s56 offen offset:1024 lds\n"
"v_mfma_f32_32x32x16_bf16 a[48:63], a[180:183], v[120:123], a[48:63]\n"
"ds_read_b128 a[140:143], v232 offset:49152\n"
"v_exp_f32 v10, v10\n"
"v_add_f32 v235, v235, v8\n"
"v_exp_f32 v11, v11\n"
"v_mfma_f32_32x32x16_bf16 a[64:79], a[184:187], v[104:107], a[64:79]\n"
"ds_read_b128 a[144:147], v229 offset:50176\n"
"v_add_f32 v236, v236, v9\n"
"v_cvt_pk_bf16_f32 v8, v8, v9\n"
"v_exp_f32 v12, v12\n"
"v_add_f32 v235, v235, v10\n"
"v_mfma_f32_32x32x16_bf16 a[80:95], a[184:187], v[120:123], a[80:95]\n"
"ds_read_b128 a[148:151], v230 offset:50176\n"
"v_exp_f32 v13, v13\n"
"v_add_f32 v236, v236, v11\n"
"v_cvt_pk_bf16_f32 v9, v10, v11\n"
"v_mfma_f32_32x32x16_bf16 a[96:111], a[188:191], v[104:107], a[96:111]\n"
"ds_read_b128 a[152:155], v231 offset:50176\n"
"v_exp_f32 v14, v14\n"
"v_add_f32 v235, v235, v12\n"
"v_exp_f32 v15, v15\n"
"v_mfma_f32_32x32x16_bf16 a[112:127], a[188:191], v[120:123], a[112:127]\n"
"s_add_u32 m0, s45, 34816\n"
"s_add_u32 s56, s49, 180224\n"
"s_and_b32 s56, s56, 0xfffff\n"
"buffer_load_dwordx4 v227, s[24:27], s56 offen lds\n"
"s_waitcnt lgkmcnt(3)\n"
"v_mfma_f32_32x32x16_bf16 v[64:79], a[128:131], v[160:163], v[128:143]\n"
"ds_read_b128 a[156:159], v232 offset:50176\n"
"buffer_load_dwordx4 v252, s[24:27], s56 offen offset:1024 lds\n"
"v_add_f32 v236, v236, v13\n"
"v_mfma_f32_32x32x16_bf16 v[80:95], a[128:131], v[192:195], v[144:159]\n"
"v_cvt_pk_bf16_f32 v10, v12, v13\n"
"v_exp_f32 v24, v24\n"
"v_add_f32 v235, v235, v14\n"
"v_exp_f32 v25, v25\n"
"v_mfma_f32_32x32x16_bf16 v[64:79], a[132:135], v[164:167], v[64:79]\n"
"ds_read_b128 a[160:163], v229 offset:57344\n"
"v_add_f32 v236, v236, v15\n"
"v_cvt_pk_bf16_f32 v11, v14, v15\n"
"v_exp_f32 v26, v26\n"
"v_add_f32 v237, v237, v24\n"
"v_mfma_f32_32x32x16_bf16 v[80:95], a[132:135], v[196:199], v[80:95]\n"
"v_exp_f32 v27, v27\n"
"v_add_f32 v238, v238, v25\n"
"v_cvt_pk_bf16_f32 v24, v24, v25\n"
"v_exp_f32 v28, v28\n"
"v_mfma_f32_32x32x16_bf16 v[64:79], a[136:139], v[168:171], v[64:79]\n"
"ds_read_b128 a[164:167], v230 offset:57344\n"
"s_cmp_lg_u32 s58, 0\n"
"s_cbranch_scc1 Lstub27_%=\n"
"Lret27_%=:\n"
"v_add_f32 v237, v237, v26\n"
"v_exp_f32 v29, v29\n"
"v_mfma_f32_32x32x16_bf16 v[80:95], a[136:139], v[200:203], v[80:95]\n"
"s_add_u32 m0, s45, 147456\n"
"s_add_u32 s56, s49, 196608\n"
"s_and_b32 s56, s56, 0xfffff\n"
"buffer_load_dwordx4 v227, s[24:27], s56 offen lds\n"
"v_mfma_f32_32x32x16_bf16 v[64:79], a[140:143], v[172:175], v[64:79]\n"
"ds_read_b128 a[168:171], v231 offset:57344\n"
"buffer_load_dwordx4 v252, s[24:27], s56 offen offset:1024 lds\n"
"v_add_f32 v238, v238, v27\n"
"v_mfma_f32_32x32x16_bf16 v[80:95], a[140:143], v[204:207], v[80:95]\n"
"v_cvt_pk_bf16_f32 v25, v26, v27\n"
"v_exp_f32 v30, v30\n"
"v_add_f32 v237, v237, v28\n"
"v_exp_f32 v31, v31\n"
"s_waitcnt lgkmcnt(2)\n"
"v_mfma_f32_32x32x16_bf16 v[64:79], a[144:147], v[176:179], v[64:79]\n"
"ds_read_b128 a[172:175], v232 offset:57344\n"
"v_add_f32 v238, v238, v29\n"
"v_cvt_pk_bf16_f32 v26, v28, v29\n"
"v_exp_f32 v32, v32\n"
"v_add_f32 v237, v237, v30\n"
"v_mfma_f32_32x32x16_bf16 v[80:95], a[144:147], v[208:211], v[80:95]\n"
"v_exp_f32 v33, v33\n"
"v_add_f32 v238, v238, v31\n"
"v_cvt_pk_bf16_f32 v27, v30, v31\n"
"v_exp_f32 v34, v34\n"
"v_mfma_f32_32x32x16_bf16 v[64:79], a[148:151], v[180:183], v[64:79]\n"
"ds_read_b128 a[176:179], v229 offset:58368\n"
"v_add_f32 v235, v235, v32\n"
"v_exp_f32 v35, v35\n"
"v_add_f32 v236, v236, v33\n"
"v_cvt_pk_bf16_f32 v32, v32, v33\n"
"v_mfma_f32_32x32x16_bf16 v[80:95], a[148:151], v[212:215], v[80:95]\n"
"s_add_u32 m0, s45, 149504\n"
"s_add_u32 s56, s49, 212992\n"
"s_and_b32 s56, s56, 0xfffff\n"
"buffer_load_dwordx4 v227, s[24:27], s56 offen lds\n"
"v_mfma_f32_32x32x16_bf16 v[64:79], a[152:155], v[184:187], v[64:79]\n"
"ds_read_b128 a[180:183], v230 offset:58368\n"
"buffer_load_dwordx4 v252, s[24:27], s56 offen offset:1024 lds\n"
"v_mfma_f32_32x32x16_bf16 v[80:95], a[152:155], v[216:219], v[80:95]\n"
"v_exp_f32 v36, v36\n"
"v_add_f32 v235, v235, v34\n"
"v_exp_f32 v37, v37\n"
"v_add_f32 v236, v236, v35\n"
"v_mfma_f32_32x32x16_bf16 v[64:79], a[156:159], v[188:191], v[64:79]\n"
"ds_read_b128 a[184:187], v231 offset:58368\n"
"v_cvt_pk_bf16_f32 v33, v34, v35\n"
"v_exp_f32 v38, v38\n"
"v_add_f32 v235, v235, v36\n"
"v_mfma_f32_32x32x16_bf16 v[80:95], a[156:159], v[220:223], v[80:95]\n"
"v_exp_f32 v39, v39\n"
"v_add_f32 v236, v236, v37\n"
"v_cvt_pk_bf16_f32 v34, v36, v37\n"
"v_exp_f32 v48, v48\n"
"v_mfma_f32_32x32x16_bf16 v[96:111], a[160:163], v[160:163], v[128:143]\n"
"ds_read_b128 a[188:191], v232 offset:58368\n"
"v_add_f32 v235, v235, v38\n"
"v_exp_f32 v49, v49\n"
"v_add_f32 v236, v236, v39\n"
"v_cvt_pk_bf16_f32 v35, v38, v39\n"
"v_mfma_f32_32x32x16_bf16 v[112:127], a[160:163], v[192:195], v[144:159]\n"
"v_exp_f32 v50, v50\n"
"v_add_f32 v237, v237, v48\n"
"v_exp_f32 v51, v51\n"
"v_add_f32 v238, v238, v49\n"
"s_waitcnt lgkmcnt(2)\n"
"v_mfma_f32_32x32x16_bf16 v[96:111], a[164:167], v[164:167], v[96:111]\n"
"ds_read_b64_tr_b16 a[128:129], v233 offset:0\n"
"v_cvt_pk_bf16_f32 v48, v48, v49\n"
"v_exp_f32 v52, v52\n"
"v_add_f32 v237, v237, v50\n"
"v_mfma_f32_32x32x16_bf16 v[112:127], a[164:167], v[196:199], v[112:127]\n"
"v_max_f32 v239, v64, v65\n"
"v_max_f32 v240, v80, v81\n"
"ds_read_b64_tr_b16 a[130:131], v233 offset:2048\n"
"v_exp_f32 v53, v53\n"
"v_add_f32 v238, v238, v51\n"
"v_mfma_f32_32x32x16_bf16 v[96:111], a[168:171], v[168:171], v[96:111]\n"
"v_max3_f32 v239, v239, v66, v67\n"
"ds_read_b64_tr_b16 a[132:133], v234 offset:0\n"
"v_cvt_pk_bf16_f32 v49, v50, v51\n"
"v_exp_f32 v54, v54\n"
"v_add_f32 v237, v237, v52\n"
"v_mfma_f32_32x32x16_bf16 v[112:127], a[168:171], v[200:203], v[112:127]\n"
"v_max3_f32 v240, v240, v82, v83\n"
"ds_read_b64_tr_b16 a[134:135], v234 offset:2048\n"
"v_exp_f32 v55, v55\n"
"v_add_f32 v238, v238, v53\n"
"v_cvt_pk_bf16_f32 v50, v52, v53\n"
"v_mfma_f32_32x32x16_bf16 v[96:111], a[172:175], v[172:175], v[96:111]\n"
"v_max3_f32 v239, v239, v68, v69\n"
"ds_read_b64_tr_b16 a[136:137], v233 offset:1024\n"
"v_exp_f32 v40, v40\n"
"v_add_f32 v237, v237, v54\n"
"v_mfma_f32_32x32x16_bf16 v[112:127], a[172:175], v[204:207], v[112:127]\n"
"v_max3_f32 v240, v240, v84, v85\n"
"v_max3_f32 v239, v239, v70, v71\n"
"ds_read_b64_tr_b16 a[138:139], v233 offset:3072\n"
"v_exp_f32 v41, v41\n"
"v_add_f32 v238, v238, v55\n"
"v_mfma_f32_32x32x16_bf16 v[96:111], a[176:179], v[176:179], v[96:111]\n"
"v_max3_f32 v240, v240, v86, v87\n"
"ds_read_b64_tr_b16 a[140:141], v234 offset:1024\n"
"v_cvt_pk_bf16_f32 v51, v54, v55\n"
"v_exp_f32 v42, v42\n"
"v_add_f32 v235, v235, v40\n"
"v_mfma_f32_32x32x16_bf16 v[112:127], a[176:179], v[208:211], v[112:127]\n"
"v_max3_f32 v239, v239, v72, v73\n"
"ds_read_b64_tr_b16 a[142:143], v234 offset:3072\n"
"v_exp_f32 v43, v43\n"
"v_add_f32 v236, v236, v41\n"
"v_cvt_pk_bf16_f32 v40, v40, v41\n"
"v_mfma_f32_32x32x16_bf16 v[96:111], a[180:183], v[180:183], v[96:111]\n"
"v_max3_f32 v240, v240, v88, v89\n"
"ds_read_b64_tr_b16 a[144:145], v233 offset:4096\n"
"v_exp_f32 v44, v44\n"
"v_add_f32 v235, v235, v42\n"
"v_mfma_f32_32x32x16_bf16 v[112:127], a[180:183], v[212:215], v[112:127]\n"
"v_max3_f32 v239, v239, v74, v75\n"
"v_max3_f32 v240, v240, v90, v91\n"
"ds_read_b64_tr_b16 a[146:147], v233 offset:6144\n"
"v_exp_f32 v45, v45\n"
"v_add_f32 v236, v236, v43\n"
"s_waitcnt lgkmcnt(4)\n"
"v_mfma_f32_32x32x16_bf16 v[96:111], a[184:187], v[184:187], v[96:111]\n"
"v_max3_f32 v239, v239, v76, v77\n"
"ds_read_b64_tr_b16 a[148:149], v234 offset:4096\n"
"v_cvt_pk_bf16_f32 v41, v42, v43\n"
"v_exp_f32 v46, v46\n"
"v_add_f32 v235, v235, v44\n"
"v_mfma_f32_32x32x16_bf16 v[112:127], a[184:187], v[216:219], v[112:127]\n"
"v_max3_f32 v240, v240, v92, v93\n"
"ds_read_b64_tr_b16 a[150:151], v234 offset:6144\n"
"v_exp_f32 v47, v47\n"
"v_add_f32 v236, v236, v45\n"
"v_cvt_pk_bf16_f32 v42, v44, v45\n"
"v_mfma_f32_32x32x16_bf16 v[96:111], a[188:191], v[188:191], v[96:111]\n"
"v_max3_f32 v239, v239, v78, v79\n"
"ds_read_b64_tr_b16 a[152:153], v233 offset:5120\n"
"v_exp_f32 v56, v56\n"
"v_add_f32 v235, v235, v46\n"
"v_mfma_f32_32x32x16_bf16 v[112:127], a[188:191], v[220:223], v[112:127]\n"
"v_max3_f32 v240, v240, v94, v95\n"
"ds_read_b64_tr_b16 a[154:155], v233 offset:7168\n"
"v_exp_f32 v57, v57\n"
"v_add_f32 v236, v236, v47\n"
"v_cvt_pk_bf16_f32 v43, v46, v47\n"
"v_mfma_f32_32x32x16_bf16 a[0:15], a[128:131], v[0:3], a[0:15]\n"
"ds_read_b64_tr_b16 a[156:157], v234 offset:5120\n"
"v_exp_f32 v58, v58\n"
"v_add_f32 v237, v237, v56\n"
"v_exp_f32 v59, v59\n"
"v_mfma_f32_32x32x16_bf16 a[16:31], a[128:131], v[16:19], a[16:31]\n"
"ds_read_b64_tr_b16 a[158:159], v234 offset:7168\n"
"s_waitcnt vmcnt(26)\n"
"ds_read_b64_tr_b16 v[160:161], v224 offset:0\n"
"ds_read_b64_tr_b16 v[162:163], v224 offset:1024\n"
"v_add_f32 v238, v238, v57\n"
"v_cvt_pk_bf16_f32 v56, v56, v57\n"
"v_mfma_f32_32x32x16_bf16 a[32:47], a[132:135], v[0:3], a[32:47]\n"
"ds_read_b64_tr_b16 a[160:161], v233 offset:8192\n"
"ds_read_b64_tr_b16 v[164:165], v225 offset:0\n"
"ds_read_b64_tr_b16 v[166:167], v225 offset:1024\n"
"v_exp_f32 v60, v60\n"
"v_add_f32 v237, v237, v58\n"
"v_mfma_f32_32x32x16_bf16 a[48:63], a[132:135], v[16:19], a[48:63]\n"
"ds_read_b64_tr_b16 a[162:163], v233 offset:10240\n"
"v_max3_f32 v239, v239, v96, v97\n"
"v_max3_f32 v240, v240, v112, v113\n"
"v_exp_f32 v61, v61\n"
"v_add_f32 v238, v238, v59\n"
"v_mfma_f32_32x32x16_bf16 a[64:79], a[136:139], v[0:3], a[64:79]\n"
"ds_read_b64_tr_b16 a[164:165], v234 offset:8192\n"
"v_max3_f32 v239, v239, v98, v99\n"
"v_max3_f32 v240, v240, v114, v115\n"
"v_cvt_pk_bf16_f32 v57, v58, v59\n"
"v_exp_f32 v62, v62\n"
"v_mfma_f32_32x32x16_bf16 a[80:95], a[136:139], v[16:19], a[80:95]\n"
"ds_read_b64_tr_b16 a[166:167], v234 offset:10240\n"
"v_max3_f32 v239, v239, v100, v101\n"
"v_max3_f32 v240, v240, v116, v117\n"
"s_waitcnt lgkmcnt(3)\n"
"s_add_u32 m0, s45, 0\n"
"s_add_u32 s56, s49, 425984\n"
"s_and_b32 s56, s56, 0xfffff\n"
"buffer_load_dwordx4 v227, s[24:27], s56 offen lds\n"
"s_waitcnt vmcnt(25)\n"
"ds_read_b64_tr_b16 v[168:169], v224 offset:2048\n"
"ds_read_b64_tr_b16 v[170:171], v224 offset:3072\n"
"v_mfma_f32_32x32x16_bf16 a[96:111], a[140:143], v[0:3], a[96:111]\n"
"ds_read_b64_tr_b16 a[168:169], v233 offset:9216\n"
"v_max3_f32 v239, v239, v102, v103\n"
"v_max3_f32 v240, v240, v118, v119\n"
"buffer_load_dwordx4 v252, s[24:27], s56 offen offset:1024 lds\n"
"ds_read_b64_tr_b16 v[172:173], v225 offset:2048\n"
"ds_read_b64_tr_b16 v[174:175], v225 offset:3072\n"
"v_mfma_f32_32x32x16_bf16 a[112:127], a[140:143], v[16:19], a[112:127]\n"
"ds_read_b64_tr_b16 a[170:171], v233 offset:11264\n"
"v_max3_f32 v239, v239, v104, v105\n"
"v_add_f32 v237, v237, v60\n"
"v_exp_f32 v63, v63\n"
"v_add_f32 v238, v238, v61\n"
"v_mfma_f32_32x32x16_bf16 a[0:15], a[144:147], v[8:11], a[0:15]\n"
"ds_read_b64_tr_b16 a[172:173], v234 offset:9216\n"
"v_max3_f32 v240, v240, v120, v121\n"
"v_max3_f32 v239, v239, v106, v107\n"
"v_cvt_pk_bf16_f32 v58, v60, v61\n"
"v_add_f32 v237, v237, v62\n"
"v_add_f32 v238, v238, v63\n"
"v_mfma_f32_32x32x16_bf16 a[16:31], a[144:147], v[24:27], a[16:31]\n"
"v_max3_f32 v240, v240, v122, v123\n"
"v_max3_f32 v239, v239, v108, v109\n"
"s_waitcnt lgkmcnt(2)\n"
"s_add_u32 m0, s45, 2048\n"
"s_add_u32 s56, s49, 442368\n"
"s_and_b32 s56, s56, 0xfffff\n"
"buffer_load_dwordx4 v227, s[24:27], s56 offen lds\n"
"s_waitcnt vmcnt(25)\n"
"ds_read_b64_tr_b16 v[176:177], v226 offset:49152\n"
"ds_read_b64_tr_b16 v[178:179], v226 offset:50176\n"
"v_mfma_f32_32x32x16_bf16 a[32:47], a[148:151], v[8:11], a[32:47]\n"
"ds_read_b64_tr_b16 a[174:175], v234 offset:11264\n"
"v_max3_f32 v240, v240, v124, v125\n"
"v_max3_f32 v239, v239, v110, v111\n"
"buffer_load_dwordx4 v252, s[24:27], s56 offen offset:1024 lds\n"
"ds_read_b64_tr_b16 v[180:181], v250 offset:49152\n"
"ds_read_b64_tr_b16 v[182:183], v250 offset:50176\n"
"v_mfma_f32_32x32x16_bf16 a[48:63], a[148:151], v[24:27], a[48:63]\n"
"ds_read_b64_tr_b16 a[176:177], v233 offset:12288\n"
"v_max3_f32 v240, v240, v126, v127\n"
"v_cvt_pk_bf16_f32 v59, v62, v63\n"
"v_mfma_f32_32x32x16_bf16 a[64:79], a[152:155], v[8:11], a[64:79]\n"
"ds_read_b64_tr_b16 a[178:179], v233 offset:14336\n"
"v_max_f32 v239, v239, v240\n"
"v_cmp_lt_f32 vcc, 0x41000000, v239\n"
"s_nop 0\n"
"s_cbranch_vccnz Lstub7_%=\n"
"Lret7_%=:\n"
"v_mfma_f32_32x32x16_bf16 a[80:95], a[152:155], v[24:27], a[80:95]\n"
"ds_read_b64_tr_b16 a[180:181], v234 offset:12288\n"
"s_waitcnt lgkmcnt(3)\n"
"s_add_u32 m0, s45, 114688\n"
"s_add_u32 s56, s49, 458752\n"
"s_and_b32 s56, s56, 0xfffff\n"
"buffer_load_dwordx4 v227, s[24:27], s56 offen lds\n"
"s_waitcnt vmcnt(25)\n"
"ds_read_b64_tr_b16 v[184:185], v224 offset:16384\n"
"ds_read_b64_tr_b16 v[186:187], v224 offset:17408\n"
"v_exp_f32 v64, v64\n"
"v_mfma_f32_32x32x16_bf16 a[96:111], a[156:159], v[8:11], a[96:111]\n"
"ds_read_b64_tr_b16 a[182:183], v234 offset:14336\n"
"buffer_load_dwordx4 v252, s[24:27], s56 offen offset:1024 lds\n"
"ds_read_b64_tr_b16 v[188:189], v225 offset:16384\n"
"ds_read_b64_tr_b16 v[190:191], v225 offset:17408\n"
"v_exp_f32 v65, v65\n"
"v_exp_f32 v66, v66\n"
"v_add_f32 v235, v235, v64\n"
"v_exp_f32 v67, v67\n"
"v_mfma_f32_32x32x16_bf16 a[112:127], a[156:159], v[24:27], a[112:127]\n"
"ds_read_b64_tr_b16 a[184:185], v233 offset:13312\n"
"v_add_f32 v236, v236, v65\n"
"v_cvt_pk_bf16_f32 v64, v64, v65\n"
"v_exp_f32 v68, v68\n"
"v_add_f32 v235, v235, v66\n"
"v_exp_f32 v69, v69\n"
"v_add_f32 v236, v236, v67\n"
"v_cvt_pk_bf16_f32 v65, v66, v67\n"
"v_exp_f32 v70, v70\n"
"v_add_f32 v235, v235, v68\n"
"v_mfma_f32_32x32x16_bf16 a[0:15], a[160:163], v[32:35], a[0:15]\n"
"ds_read_b64_tr_b16 a[186:187], v233 offset:15360\n"
"v_exp_f32 v71, v71\n"
"v_add_f32 v236, v236, v69\n"
"v_cvt_pk_bf16_f32 v66, v68, v69\n"
"v_exp_f32 v80, v80\n"
"v_add_f32 v235, v235, v70\n"
"v_exp_f32 v81, v81\n"
"v_add_f32 v236, v236, v71\n"
"v_cvt_pk_bf16_f32 v67, v70, v71\n"
"v_exp_f32 v82, v82\n"
"v_mfma_f32_32x32x16_bf16 a[16:31], a[160:163], v[48:51], a[16:31]\n"
"ds_read_b64_tr_b16 a[188:189], v234 offset:13312\n"
"s_waitcnt lgkmcnt(3)\n"
"s_add_u32 m0, s45, 16384\n"
"s_add_u32 s56, s49, 475136\n"
"s_and_b32 s56, s56, 0xfffff\n"
"buffer_load_dwordx4 v227, s[24:27], s56 offen lds\n"
"s_waitcnt vmcnt(25)\n"
"ds_read_b64_tr_b16 v[192:193], v226 offset:51200\n"
"ds_read_b64_tr_b16 v[194:195], v226 offset:52224\n"
"v_add_f32 v237, v237, v80\n"
"v_mfma_f32_32x32x16_bf16 a[32:47], a[164:167], v[32:35], a[32:47]\n"
"ds_read_b64_tr_b16 a[190:191], v234 offset:15360\n"
"buffer_load_dwordx4 v252, s[24:27], s56 offen offset:1024 lds\n"
"ds_read_b64_tr_b16 v[196:197], v250 offset:51200\n"
"ds_read_b64_tr_b16 v[198:199], v250 offset:52224\n"
"v_exp_f32 v83, v83\n"
"v_add_f32 v238, v238, v81\n"
"v_cvt_pk_bf16_f32 v80, v80, v81\n"
"v_exp_f32 v84, v84\n"
"v_add_f32 v237, v237, v82\n"
"v_mfma_f32_32x32x16_bf16 a[48:63], a[164:167], v[48:51], a[48:63]\n"
"v_exp_f32 v85, v85\n"
"v_add_f32 v238, v238, v83\n"
"v_cvt_pk_bf16_f32 v81, v82, v83\n"
"v_exp_f32 v86, v86\n"
"v_add_f32 v237, v237, v84\n"
"v_exp_f32 v87, v87\n"
"v_add_f32 v238, v238, v85\n"
"v_cvt_pk_bf16_f32 v82, v84, v85\n"
"v_exp_f32 v72, v72\n"
"v_add_f32 v237, v237, v86\n"
"v_mfma_f32_32x32x16_bf16 a[64:79], a[168:171], v[32:35], a[64:79]\n"
"s_mov_b64 exec, s[46:47]\n"
"buffer_load_dword v249, v245, s[40:43], 0 offen sc1\n"
"s_mov_b64 exec, -1\n"
"v_exp_f32 v73, v73\n"
"v_add_f32 v238, v238, v87\n"
"v_cvt_pk_bf16_f32 v83, v86, v87\n"
"v_exp_f32 v74, v74\n"
"v_add_f32 v235, v235, v72\n"
"v_mfma_f32_32x32x16_bf16 a[80:95], a[168:171], v[48:51], a[80:95]\n"
"s_waitcnt lgkmcnt(0)\n"
"s_add_u32 m0, s45, 116736\n"
"s_add_u32 s56, s49, 491520\n"
"s_and_b32 s56, s56, 0xfffff\n"
"buffer_load_dwordx4 v227, s[24:27], s56 offen lds\n"
"s_waitcnt vmcnt(26)\n"
"ds_read_b64_tr_b16 v[200:201], v224 offset:18432\n"
"ds_read_b64_tr_b16 v[202:203], v224 offset:19456\n"
"v_exp_f32 v75, v75\n"
"v_add_f32 v236, v236, v73\n"
"v_mfma_f32_32x32x16_bf16 a[96:111], a[172:175], v[32:35], a[96:111]\n"
"buffer_load_dwordx4 v252, s[24:27], s56 offen offset:1024 lds\n"
"ds_read_b64_tr_b16 v[204:205], v225 offset:18432\n"
"ds_read_b64_tr_b16 v[206:207], v225 offset:19456\n"
"v_cvt_pk_bf16_f32 v72, v72, v73\n"
"v_exp_f32 v76, v76\n"
"v_add_f32 v235, v235, v74\n"
"v_exp_f32 v77, v77\n"
"v_add_f32 v236, v236, v75\n"
"v_cvt_pk_bf16_f32 v73, v74, v75\n"
"v_mfma_f32_32x32x16_bf16 a[112:127], a[172:175], v[48:51], a[112:127]\n"
"v_exp_f32 v78, v78\n"
"v_add_f32 v235, v235, v76\n"
"v_exp_f32 v79, v79\n"
"v_add_f32 v236, v236, v77\n"
"v_cvt_pk_bf16_f32 v74, v76, v77\n"
"v_exp_f32 v88, v88\n"
"v_add_f32 v235, v235, v78\n"
"v_exp_f32 v89, v89\n"
"v_add_f32 v236, v236, v79\n"
"v_cvt_pk_bf16_f32 v75, v78, v79\n"
"v_mfma_f32_32x32x16_bf16 a[0:15], a[176:179], v[40:43], a[0:15]\n"
"s_waitcnt lgkmcnt(0)\n"
"s_barrier\n"
"ds_read_b64_tr_b16 a[128:129], v233 offset:16384\n"
"ds_read_b64_tr_b16 a[130:131], v233 offset:18432\n"
"v_exp_f32 v90, v90\n"
"v_add_f32 v237, v237, v88\n"
"v_exp_f32 v91, v91\n"
"v_add_f32 v238, v238, v89\n"
"v_cvt_pk_bf16_f32 v88, v88, v89\n"
"v_exp_f32 v92, v92\n"
"v_add_f32 v237, v237, v90\n"
"v_mfma_f32_32x32x16_bf16 a[16:31], a[176:179], v[56:59], a[16:31]\n"
"ds_read_b64_tr_b16 a[132:133], v234 offset:16384\n"
"ds_read_b64_tr_b16 a[134:135], v234 offset:18432\n"
"s_add_u32 m0, s45, 49152\n"
"s_add_u32 s56, s49, 229376\n"
"s_and_b32 s56, s56, 0xfffff\n"
"buffer_load_dwordx4 v227, s[24:27], s56 offen lds\n"
"s_waitcnt vmcnt(26)\n"
"ds_read_b64_tr_b16 v[208:209], v251 offset:0\n"
"ds_read_b64_tr_b16 v[210:211], v251 offset:1024\n"
"v_exp_f32 v93, v93\n"
"v_mfma_f32_32x32x16_bf16 a[32:47], a[180:183], v[40:43], a[32:47]\n"
"ds_read_b64_tr_b16 a[136:137], v233 offset:17408\n"
"ds_read_b64_tr_b16 a[138:139], v233 offset:19456\n"
"buffer_load_dwordx4 v252, s[24:27], s56 offen offset:1024 lds\n"
"ds_read_b64_tr_b16 v[212:213], v255 offset:0\n"
"ds_read_b64_tr_b16 v[214:215], v255 offset:1024\n"
"v_add_f32 v238, v238, v91\n"
"v_cvt_pk_bf16_f32 v89, v90, v91\n"
"v_exp_f32 v94, v94\n"
"v_add_f32 v237, v237, v92\n"
"v_mfma_f32_32x32x16_bf16 a[48:63], a[180:183], v[56:59], a[48:63]\n"
"ds_read_b64_tr_b16 a[140:141], v234 offset:17408\n"
"s_add_u32 m0, s45, 18432\n"
"s_add_u32 s56, s49, 507904\n"
"s_and_b32 s56, s56, 0xfffff\n"
"buffer_load_dwordx4 v227, s[24:27], s56 offen lds\n"
"v_exp_f32 v95, v95\n"
"v_add_f32 v238, v238, v93\n"
"v_cvt_pk_bf16_f32 v90, v92, v93\n"
"v_mfma_f32_32x32x16_bf16 a[64:79], a[184:187], v[40:43], a[64:79]\n"
"ds_read_b64_tr_b16 a[142:143], v234 offset:19456\n"
"ds_read_b64_tr_b16 a[144:145], v233 offset:20480\n"
"buffer_load_dwordx4 v252, s[24:27], s56 offen offset:1024 lds\n"
"v_exp_f32 v96, v96\n"
"v_add_f32 v237, v237, v94\n"
"v_exp_f32 v97, v97\n"
"v_add_f32 v238, v238, v95\n"
"v_cvt_pk_bf16_f32 v91, v94, v95\n"
"v_mfma_f32_32x32x16_bf16 a[80:95], a[184:187], v[56:59], a[80:95]\n"
"ds_read_b64_tr_b16 a[146:147], v233 offset:22528\n"
"ds_read_b64_tr_b16 a[148:149], v234 offset:20480\n"
"s_waitcnt lgkmcnt(5)\n"
"s_add_u32 m0, s45, 131072\n"
"s_add_u32 s56, s49, 524288\n"
"s_and_b32 s56, s56, 0xfffff\n"
"buffer_load_dwordx4 v227, s[24:27], s56 offen lds\n"
"s_waitcnt vmcnt(28)\n"
"ds_read_b64_tr_b16 v[216:217], v226 offset:32768\n"
"ds_read_b64_tr_b16 v[218:219], v226 offset:33792\n"
"v_mfma_f32_32x32x16_bf16 a[96:111], a[188:191], v[40:43], a[96:111]\n"
"ds_read_b64_tr_b16 a[150:151], v234 offset:22528\n"
"ds_read_b64_tr_b16 a[152:153], v233 offset:21504\n"
"buffer_load_dwordx4 v252, s[24:27], s56 offen offset:1024 lds\n"
"ds_read_b64_tr_b16 v[220:221], v250 offset:32768\n"
"ds_read_b64_tr_b16 v[222:223], v250 offset:33792\n"
"v_exp_f32 v98, v98\n"
"v_mfma_f32_32x32x16_bf16 a[112:127], a[188:191], v[56:59], a[112:127]\n"
"ds_read_b64_tr_b16 a[154:155], v233 offset:23552\n"
"s_add_u32 m0, s45, 51200\n"
"s_add_u32 s56, s49, 245760\n"
"s_and_b32 s56, s56, 0xfffff\n"
"buffer_load_dwordx4 v227, s[24:27], s56 offen lds\n"
"s_cmp_lg_u32 s58, 0\n"
"s_cbranch_scc1 Lstub28_%=\n"
"Lret28_%=:\n"
"v_mfma_f32_32x32x16_bf16 a[0:15], a[128:131], v[64:67], a[0:15]\n"
"ds_read_b64_tr_b16 a[156:157], v234 offset:21504\n"
"ds_read_b64_tr_b16 a[158:159], v234 offset:23552\n"
"buffer_load_dwordx4 v252, s[24:27], s56 offen offset:1024 lds\n"
"v_add_f32 v235, v235, v96\n"
"v_exp_f32 v99, v99\n"
"v_add_f32 v236, v236, v97\n"
"v_cvt_pk_bf16_f32 v96, v96, v97\n"
"v_mfma_f32_32x32x16_bf16 a[16:31], a[128:131], v[80:83], a[16:31]\n"
"ds_read_b64_tr_b16 a[160:161], v233 offset:24576\n"
"s_waitcnt lgkmcnt(4)\n"
"s_add_u32 m0, s45, 98304\n"
"s_add_u32 s56, s49, 540672\n"
"s_and_b32 s56, s56, 0xfffff\n"
"buffer_load_dwordx4 v227, s[24:27], s56 offen lds\n"
"v_exp_f32 v100, v100\n"
"v_mfma_f32_32x32x16_bf16 a[32:47], a[132:135], v[64:67], a[32:47]\n"
"ds_read_b64_tr_b16 a[162:163], v233 offset:26624\n"
"buffer_load_dwordx4 v252, s[24:27], s56 offen offset:1024 lds\n"
"v_add_f32 v235, v235, v98\n"
"v_exp_f32 v101, v101\n"
"v_add_f32 v236, v236, v99\n"
"v_cvt_pk_bf16_f32 v97, v98, v99\n"
"v_mfma_f32_32x32x16_bf16 a[48:63], a[132:135], v[80:83], a[48:63]\n"
"ds_read_b64_tr_b16 a[164:165], v233 offset:28672\n"
"v_exp_f32 v102, v102\n"
"v_add_f32 v235, v235, v100\n"
"v_exp_f32 v103, v103\n"
"v_add_f32 v236, v236, v101\n"
"v_cvt_pk_bf16_f32 v98, v100, v101\n"
"v_exp_f32 v112, v112\n"
"v_add_f32 v235, v235, v102\n"
"v_mfma_f32_32x32x16_bf16 a[64:79], a[136:139], v[64:67], a[64:79]\n"
"ds_read_b64_tr_b16 a[166:167], v233 offset:30720\n"
"v_exp_f32 v113, v113\n"
"v_add_f32 v236, v236, v103\n"
"v_cvt_pk_bf16_f32 v99, v102, v103\n"
"v_exp_f32 v114, v114\n"
"v_add_f32 v237, v237, v112\n"
"v_exp_f32 v115, v115\n"
"v_add_f32 v238, v238, v113\n"
"v_mfma_f32_32x32x16_bf16 a[80:95], a[136:139], v[80:83], a[80:95]\n"
"ds_read_b64_tr_b16 a[168:169], v234 offset:24576\n"
"s_add_u32 m0, s45, 65536\n"
"s_add_u32 s56, s49, 262144\n"
"s_and_b32 s56, s56, 0xfffff\n"
"buffer_load_dwordx4 v227, s[24:27], s56 offen lds\n"
"v_cvt_pk_bf16_f32 v112, v112, v113\n"
"v_exp_f32 v116, v116\n"
"v_mfma_f32_32x32x16_bf16 a[96:111], a[140:143], v[64:67], a[96:111]\n"
"ds_read_b64_tr_b16 a[170:171], v234 offset:26624\n"
"buffer_load_dwordx4 v252, s[24:27], s56 offen offset:1024 lds\n"
"v_add_f32 v237, v237, v114\n"
"v_exp_f32 v117, v117\n"
"v_add_f32 v238, v238, v115\n"
"v_cvt_pk_bf16_f32 v113, v114, v115\n"
"v_mfma_f32_32x32x16_bf16 a[112:127], a[140:143], v[80:83], a[112:127]\n"
"ds_read_b64_tr_b16 a[172:173], v234 offset:28672\n"
"v_exp_f32 v118, v118\n"
"v_add_f32 v237, v237, v116\n"
"v_exp_f32 v119, v119\n"
"v_add_f32 v238, v238, v117\n"
"v_cvt_pk_bf16_f32 v114, v116, v117\n"
"v_exp_f32 v104, v104\n"
"v_add_f32 v237, v237, v118\n"
"v_mfma_f32_32x32x16_bf16 a[0:15], a[144:147], v[72:75], a[0:15]\n"
"ds_read_b64_tr_b16 a[174:175], v234 offset:30720\n"
"ds_read_b64_tr_b16 a[176:177], v233 offset:25600\n"
"v_exp_f32 v105, v105\n"
"v_add_f32 v238, v238, v119\n"
"v_cvt_pk_bf16_f32 v115, v118, v119\n"
"v_exp_f32 v106, v106\n"
"v_add_f32 v235, v235, v104\n"
"v_exp_f32 v107, v107\n"
"v_mfma_f32_32x32x16_bf16 a[16:31], a[144:147], v[88:91], a[16:31]\n"
"ds_read_b64_tr_b16 a[178:179], v233 offset:27648\n"
"v_add_f32 v236, v236, v105\n"
"v_cvt_pk_bf16_f32 v104, v104, v105\n"
"v_exp_f32 v108, v108\n"
"v_add_f32 v235, v235, v106\n"
"v_exp_f32 v109, v109\n"
"v_add_f32 v236, v236, v107\n"
"v_cvt_pk_bf16_f32 v105, v106, v107\n"
"v_mfma_f32_32x32x16_bf16 a[32:47], a[148:151], v[72:75], a[32:47]\n"
"ds_read_b64_tr_b16 a[180:181], v233 offset:29696\n"
"v_exp_f32 v110, v110\n"
"v_add_f32 v235, v235, v108\n"
"v_exp_f32 v111, v111\n"
"v_add_f32 v236, v236, v109\n"
"v_cvt_pk_bf16_f32 v106, v108, v109\n"
"v_exp_f32 v120, v120\n"
"v_add_f32 v235, v235, v110\n"
"v_mfma_f32_32x32x16_bf16 a[48:63], a[148:151], v[88:91], a[48:63]\n"
"ds_read_b64_tr_b16 a[182:183], v233 offset:31744\n"
"s_add_u32 m0, s45, 67584\n"
"s_add_u32 s56, s49, 278528\n"
"s_and_b32 s56, s56, 0xfffff\n"
"buffer_load_dwordx4 v227, s[24:27], s56 offen lds\n"
"v_exp_f32 v121, v121\n"
"v_add_f32 v236, v236, v111\n"
"s_waitcnt lgkmcnt(2)\n"
"v_mfma_f32_32x32x16_bf16 a[64:79], a[152:155], v[72:75], a[64:79]\n"
"ds_read_b64_tr_b16 a[184:185], v234 offset:25600\n"
"buffer_load_dwordx4 v252, s[24:27], s56 offen offset:1024 lds\n"
"v_cvt_pk_bf16_f32 v107, v110, v111\n"
"v_exp_f32 v122, v122\n"
"v_add_f32 v237, v237, v120\n"
"v_exp_f32 v123, v123\n"
"v_mfma_f32_32x32x16_bf16 a[80:95], a[152:155], v[88:91], a[80:95]\n"
"ds_read_b64_tr_b16 a[186:187], v234 offset:27648\n"
"v_add_f32 v238, v238, v121\n"
"v_cvt_pk_bf16_f32 v120, v120, v121\n"
"v_exp_f32 v124, v124\n"
"v_add_f32 v237, v237, v122\n"
"v_exp_f32 v125, v125\n"
"v_add_f32 v238, v238, v123\n"
"v_cvt_pk_bf16_f32 v121, v122, v123\n"
"v_mfma_f32_32x32x16_bf16 a[96:111], a[156:159], v[72:75], a[96:111]\n"
"ds_read_b64_tr_b16 a[188:189], v234 offset:29696\n"
"v_exp_f32 v126, v126\n"
"v_add_f32 v237, v237, v124\n"
"v_exp_f32 v127, v127\n"
"v_add_f32 v238, v238, v125\n"
"v_cvt_pk_bf16_f32 v122, v124, v125\n"
"v_add_f32 v237, v237, v126\n"
"v_add_f32 v238, v238, v127\n"
"v_cvt_pk_bf16_f32 v123, v126, v127\n"
"v_mfma_f32_32x32x16_bf16 a[112:127], a[156:159], v[88:91], a[112:127]\n"
"ds_read_b64_tr_b16 a[190:191], v234 offset:31744\n"
"v_add_f32 v235, v235, v236\n"
"v_mov_b32 v236, v235\n"
"s_nop 1\n"
"v_permlane32_swap_b32 v235, v236\n"
"v_add_f32 v235, v235, v236\n"
"v_rcp_f32 v235, v235\n"
"s_nop 0\n"
"v_mov_b32 v240, v235\n"
"v_mov_b32 v241, v235\n"
"v_add_f32 v237, v237, v238\n"
"v_mov_b32 v238, v237\n"
"s_nop 1\n"
"v_permlane32_swap_b32 v237, v238\n"
"v_add_f32 v237, v237, v238\n"
"v_rcp_f32 v237, v237\n"
"s_nop 0\n"
"v_mov_b32 v242, v237\n"
"v_mov_b32 v243, v237\n"
"v_mfma_f32_32x32x16_bf16 a[0:15], a[160:163], v[96:99], a[0:15]\n"
"v_mfma_f32_32x32x16_bf16 a[0:15], a[164:167], v[104:107], a[0:15]\n"
"v_mfma_f32_32x32x16_bf16 a[16:31], a[160:163], v[112:115], a[16:31]\n"
"v_mfma_f32_32x32x16_bf16 a[16:31], a[164:167], v[120:123], a[16:31]\n"
"s_mov_b32 s57, 0\n"
"s_nop 8\n"
"v_accvgpr_read_b32 v0, a0\n"
"v_accvgpr_read_b32 v1, a1\n"
"v_accvgpr_read_b32 v2, a2\n"
"v_accvgpr_read_b32 v3, a3\n"
"v_accvgpr_read_b32 v4, a4\n"
"v_accvgpr_read_b32 v5, a5\n"
"v_accvgpr_read_b32 v6, a6\n"
"v_accvgpr_read_b32 v7, a7\n"
"v_accvgpr_read_b32 v8, a8\n"
"v_accvgpr_read_b32 v9, a9\n"
"v_accvgpr_read_b32 v10, a10\n"
"v_accvgpr_read_b32 v11, a11\n"
"v_accvgpr_read_b32 v12, a12\n"
"v_accvgpr_read_b32 v13, a13\n"
"v_accvgpr_read_b32 v14, a14\n"
"v_accvgpr_read_b32 v15, a15\n"
"v_pk_mul_f32 v[0:1], v[0:1], v[240:241]\n"
"v_mfma_f32_32x32x16_bf16 a[32:47], a[168:171], v[96:99], a[32:47]\n"
"v_pk_mul_f32 v[2:3], v[2:3], v[240:241]\n"
"v_pk_mul_f32 v[4:5], v[4:5], v[240:241]\n"
"v_pk_mul_f32 v[6:7], v[6:7], v[240:241]\n"
"v_pk_mul_f32 v[8:9], v[8:9], v[240:241]\n"
"v_pk_mul_f32 v[10:11], v[10:11], v[240:241]\n"
"v_pk_mul_f32 v[12:13], v[12:13], v[240:241]\n"
"v_pk_mul_f32 v[14:15], v[14:15], v[240:241]\n"
"v_cvt_pk_bf16_f32 v0, v0, v1\n"
"v_cvt_pk_bf16_f32 v1, v2, v3\n"
"v_cvt_pk_bf16_f32 v2, v4, v5\n"
"v_cvt_pk_bf16_f32 v3, v6, v7\n"
"v_cvt_pk_bf16_f32 v4, v8, v9\n"
"v_cvt_pk_bf16_f32 v5, v10, v11\n"
"v_cvt_pk_bf16_f32 v6, v12, v13\n"
"v_cvt_pk_bf16_f32 v7, v14, v15\n"
"buffer_store_dwordx4 v[0:3], v228, s[28:31], s57 offen offset:0 sc1\n"
"buffer_store_dwordx4 v[4:7], v228, s[28:31], s57 offen offset:2048 sc1\n"
"v_mfma_f32_32x32x16_bf16 a[32:47], a[172:175], v[104:107], a[32:47]\n"
"v_accvgpr_read_b32 v16, a16\n"
"v_accvgpr_read_b32 v17, a17\n"
"v_accvgpr_read_b32 v18, a18\n"
"v_accvgpr_read_b32 v19, a19\n"
"v_accvgpr_read_b32 v20, a20\n"
"v_accvgpr_read_b32 v21, a21\n"
"v_accvgpr_read_b32 v22, a22\n"
"v_accvgpr_read_b32 v23, a23\n"
"v_accvgpr_read_b32 v24, a24\n"
"v_accvgpr_read_b32 v25, a25\n"
"v_accvgpr_read_b32 v26, a26\n"
"v_accvgpr_read_b32 v27, a27\n"
"v_accvgpr_read_b32 v28, a28\n"
"v_accvgpr_read_b32 v29, a29\n"
"v_accvgpr_read_b32 v30, a30\n"
"v_accvgpr_read_b32 v31, a31\n"
"v_pk_mul_f32 v[16:17], v[16:17], v[242:243]\n"
"v_pk_mul_f32 v[18:19], v[18:19], v[242:243]\n"
"v_mfma_f32_32x32x16_bf16 a[48:63], a[168:171], v[112:115], a[48:63]\n"
"v_pk_mul_f32 v[20:21], v[20:21], v[242:243]\n"
"v_pk_mul_f32 v[22:23], v[22:23], v[242:243]\n"
"v_pk_mul_f32 v[24:25], v[24:25], v[242:243]\n"
"v_pk_mul_f32 v[26:27], v[26:27], v[242:243]\n"
"v_pk_mul_f32 v[28:29], v[28:29], v[242:243]\n"
"v_pk_mul_f32 v[30:31], v[30:31], v[242:243]\n"
"v_cvt_pk_bf16_f32 v16, v16, v17\n"
"v_cvt_pk_bf16_f32 v17, v18, v19\n"
"v_cvt_pk_bf16_f32 v18, v20, v21\n"
"v_cvt_pk_bf16_f32 v19, v22, v23\n"
"v_cvt_pk_bf16_f32 v20, v24, v25\n"
"v_cvt_pk_bf16_f32 v21, v26, v27\n"
"v_cvt_pk_bf16_f32 v22, v28, v29\n"
"v_cvt_pk_bf16_f32 v23, v30, v31\n"
"buffer_store_dwordx4 v[16:19], v228, s[28:31], s57 offen offset:1024 sc1\n"
"buffer_store_dwordx4 v[20:23], v228, s[28:31], s57 offen offset:3072 sc1\n"
"v_mfma_f32_32x32x16_bf16 a[48:63], a[172:175], v[120:123], a[48:63]\n"
"s_mov_b32 s57, 4096\n"
"v_accvgpr_read_b32 v32, a32\n"
"v_accvgpr_read_b32 v33, a33\n"
"v_accvgpr_read_b32 v34, a34\n"
"v_accvgpr_read_b32 v35, a35\n"
"v_accvgpr_read_b32 v36, a36\n"
"v_accvgpr_read_b32 v37, a37\n"
"v_accvgpr_read_b32 v38, a38\n"
"v_accvgpr_read_b32 v39, a39\n"
"v_accvgpr_read_b32 v40, a40\n"
"v_accvgpr_read_b32 v41, a41\n"
"v_accvgpr_read_b32 v42, a42\n"
"v_accvgpr_read_b32 v43, a43\n"
"v_accvgpr_read_b32 v44, a44\n"
"v_accvgpr_read_b32 v45, a45\n"
"v_accvgpr_read_b32 v46, a46\n"
"v_accvgpr_read_b32 v47, a47\n"
"v_pk_mul_f32 v[32:33], v[32:33], v[240:241]\n"
"v_mfma_f32_32x32x16_bf16 a[64:79], a[176:179], v[96:99], a[64:79]\n"
"v_pk_mul_f32 v[34:35], v[34:35], v[240:241]\n"
"v_pk_mul_f32 v[36:37], v[36:37], v[240:241]\n"
"v_pk_mul_f32 v[38:39], v[38:39], v[240:241]\n"
"v_pk_mul_f32 v[40:41], v[40:41], v[240:241]\n"
"v_pk_mul_f32 v[42:43], v[42:43], v[240:241]\n"
"v_pk_mul_f32 v[44:45], v[44:45], v[240:241]\n"
"v_pk_mul_f32 v[46:47], v[46:47], v[240:241]\n"
"v_cvt_pk_bf16_f32 v32, v32, v33\n"
"v_cvt_pk_bf16_f32 v33, v34, v35\n"
"v_cvt_pk_bf16_f32 v34, v36, v37\n"
"v_cvt_pk_bf16_f32 v35, v38, v39\n"
"v_cvt_pk_bf16_f32 v36, v40, v41\n"
"v_cvt_pk_bf16_f32 v37, v42, v43\n"
"v_cvt_pk_bf16_f32 v38, v44, v45\n"
"v_cvt_pk_bf16_f32 v39, v46, v47\n"
"buffer_store_dwordx4 v[32:35], v228, s[28:31], s57 offen offset:0 sc1\n"
"buffer_store_dwordx4 v[36:39], v228, s[28:31], s57 offen offset:2048 sc1\n"
"s_waitcnt lgkmcnt(0)\n"
"v_mfma_f32_32x32x16_bf16 a[64:79], a[180:183], v[104:107], a[64:79]\n"
"v_accvgpr_read_b32 v48, a48\n"
"v_accvgpr_read_b32 v49, a49\n"
"v_accvgpr_read_b32 v50, a50\n"
"v_accvgpr_read_b32 v51, a51\n"
"v_accvgpr_read_b32 v52, a52\n"
"v_accvgpr_read_b32 v53, a53\n"
"v_accvgpr_read_b32 v54, a54\n"
"v_accvgpr_read_b32 v55, a55\n"
"v_accvgpr_read_b32 v56, a56\n"
"v_accvgpr_read_b32 v57, a57\n"
"v_accvgpr_read_b32 v58, a58\n"
"v_accvgpr_read_b32 v59, a59\n"
"v_accvgpr_read_b32 v60, a60\n"
"v_accvgpr_read_b32 v61, a61\n"
"v_accvgpr_read_b32 v62, a62\n"
"v_accvgpr_read_b32 v63, a63\n"
"v_pk_mul_f32 v[48:49], v[48:49], v[242:243]\n"
"v_pk_mul_f32 v[50:51], v[50:51], v[242:243]\n"
"v_mfma_f32_32x32x16_bf16 a[80:95], a[176:179], v[112:115], a[80:95]\n"
"v_pk_mul_f32 v[52:53], v[52:53], v[242:243]\n"
"v_pk_mul_f32 v[54:55], v[54:55], v[242:243]\n"
"v_pk_mul_f32 v[56:57], v[56:57], v[242:243]\n"
"v_pk_mul_f32 v[58:59], v[58:59], v[242:243]\n"
"v_pk_mul_f32 v[60:61], v[60:61], v[242:243]\n"
"v_pk_mul_f32 v[62:63], v[62:63], v[242:243]\n"
"v_cvt_pk_bf16_f32 v48, v48, v49\n"
"v_cvt_pk_bf16_f32 v49, v50, v51\n"
"v_cvt_pk_bf16_f32 v50, v52, v53\n"
"v_cvt_pk_bf16_f32 v51, v54, v55\n"
"v_cvt_pk_bf16_f32 v52, v56, v57\n"
"v_cvt_pk_bf16_f32 v53, v58, v59\n"
"v_cvt_pk_bf16_f32 v54, v60, v61\n"
"v_cvt_pk_bf16_f32 v55, v62, v63\n"
"buffer_store_dwordx4 v[48:51], v228, s[28:31], s57 offen offset:1024 sc1\n"
"buffer_store_dwordx4 v[52:55], v228, s[28:31], s57 offen offset:3072 sc1\n"
"v_mfma_f32_32x32x16_bf16 a[80:95], a[180:183], v[120:123], a[80:95]\n"
"s_mov_b32 s57, 8192\n"
"v_accvgpr_read_b32 v0, a64\n"
"v_accvgpr_read_b32 v1, a65\n"
"v_accvgpr_read_b32 v2, a66\n"
"v_accvgpr_read_b32 v3, a67\n"
"v_accvgpr_read_b32 v4, a68\n"
"v_accvgpr_read_b32 v5, a69\n"
"v_accvgpr_read_b32 v6, a70\n"
"v_accvgpr_read_b32 v7, a71\n"
"v_accvgpr_read_b32 v8, a72\n"
"v_accvgpr_read_b32 v9, a73\n"
"v_accvgpr_read_b32 v10, a74\n"
"v_accvgpr_read_b32 v11, a75\n"
"v_accvgpr_read_b32 v12, a76\n"
"v_accvgpr_read_b32 v13, a77\n"
"v_accvgpr_read_b32 v14, a78\n"
"v_accvgpr_read_b32 v15, a79\n"
"v_pk_mul_f32 v[0:1], v[0:1], v[240:241]\n"
"v_mfma_f32_32x32x16_bf16 a[96:111], a[184:187], v[96:99], a[96:111]\n"
"v_pk_mul_f32 v[2:3], v[2:3], v[240:241]\n"
"v_pk_mul_f32 v[4:5], v[4:5], v[240:241]\n"
"v_pk_mul_f32 v[6:7], v[6:7], v[240:241]\n"
"v_pk_mul_f32 v[8:9], v[8:9], v[240:241]\n"
"v_pk_mul_f32 v[10:11], v[10:11], v[240:241]\n"
"v_pk_mul_f32 v[12:13], v[12:13], v[240:241]\n"
"v_pk_mul_f32 v[14:15], v[14:15], v[240:241]\n"
"v_cvt_pk_bf16_f32 v0, v0, v1\n"
"v_cvt_pk_bf16_f32 v1, v2, v3\n"
"v_cvt_pk_bf16_f32 v2, v4, v5\n"
"v_cvt_pk_bf16_f32 v3, v6, v7\n"
"v_cvt_pk_bf16_f32 v4, v8, v9\n"
"v_cvt_pk_bf16_f32 v5, v10, v11\n"
"v_cvt_pk_bf16_f32 v6, v12, v13\n"
"v_cvt_pk_bf16_f32 v7, v14, v15\n"
"buffer_store_dwordx4 v[0:3], v228, s[28:31], s57 offen offset:0 sc1\n"
"buffer_store_dwordx4 v[4:7], v228, s[28:31], s57 offen offset:2048 sc1\n"
"v_mfma_f32_32x32x16_bf16 a[96:111], a[188:191], v[104:107], a[96:111]\n"
"v_accvgpr_read_b32 v16, a80\n"
"v_accvgpr_read_b32 v17, a81\n"
"v_accvgpr_read_b32 v18, a82\n"
"v_accvgpr_read_b32 v19, a83\n"
"v_accvgpr_read_b32 v20, a84\n"
"v_accvgpr_read_b32 v21, a85\n"
"v_accvgpr_read_b32 v22, a86\n"
"v_accvgpr_read_b32 v23, a87\n"
"v_accvgpr_read_b32 v24, a88\n"
"v_accvgpr_read_b32 v25, a89\n"
"v_accvgpr_read_b32 v26, a90\n"
"v_accvgpr_read_b32 v27, a91\n"
"v_accvgpr_read_b32 v28, a92\n"
"v_accvgpr_read_b32 v29, a93\n"
"v_accvgpr_read_b32 v30, a94\n"
"v_accvgpr_read_b32 v31, a95\n"
"v_pk_mul_f32 v[16:17], v[16:17], v[242:243]\n"
"v_pk_mul_f32 v[18:19], v[18:19], v[242:243]\n"
"v_mfma_f32_32x32x16_bf16 a[112:127], a[184:187], v[112:115], a[112:127]\n"
"v_pk_mul_f32 v[20:21], v[20:21], v[242:243]\n"
"v_pk_mul_f32 v[22:23], v[22:23], v[242:243]\n"
"v_pk_mul_f32 v[24:25], v[24:25], v[242:243]\n"
"v_pk_mul_f32 v[26:27], v[26:27], v[242:243]\n"
"v_pk_mul_f32 v[28:29], v[28:29], v[242:243]\n"
"v_pk_mul_f32 v[30:31], v[30:31], v[242:243]\n"
"v_cvt_pk_bf16_f32 v16, v16, v17\n"
"v_cvt_pk_bf16_f32 v17, v18, v19\n"
"v_cvt_pk_bf16_f32 v18, v20, v21\n"
"v_cvt_pk_bf16_f32 v19, v22, v23\n"
"v_cvt_pk_bf16_f32 v20, v24, v25\n"
"v_cvt_pk_bf16_f32 v21, v26, v27\n"
"v_cvt_pk_bf16_f32 v22, v28, v29\n"
"v_cvt_pk_bf16_f32 v23, v30, v31\n"
"buffer_store_dwordx4 v[16:19], v228, s[28:31], s57 offen offset:1024 sc1\n"
"buffer_store_dwordx4 v[20:23], v228, s[28:31], s57 offen offset:3072 sc1\n"
"v_mfma_f32_32x32x16_bf16 a[112:127], a[188:191], v[120:123], a[112:127]\n"
"s_mov_b32 s57, 12288\n"
"v_accvgpr_read_b32 v32, a96\n"
"v_accvgpr_read_b32 v33, a97\n"
"v_accvgpr_read_b32 v34, a98\n"
"v_accvgpr_read_b32 v35, a99\n"
"v_accvgpr_read_b32 v36, a100\n"
"v_accvgpr_read_b32 v37, a101\n"
"v_accvgpr_read_b32 v38, a102\n"
"v_accvgpr_read_b32 v39, a103\n"
"v_accvgpr_read_b32 v40, a104\n"
"v_accvgpr_read_b32 v41, a105\n"
"v_accvgpr_read_b32 v42, a106\n"
"v_accvgpr_read_b32 v43, a107\n"
"v_accvgpr_read_b32 v44, a108\n"
"v_accvgpr_read_b32 v45, a109\n"
"v_accvgpr_read_b32 v46, a110\n"
"v_accvgpr_read_b32 v47, a111\n"
"v_pk_mul_f32 v[32:33], v[32:33], v[240:241]\n"
"v_pk_mul_f32 v[34:35], v[34:35], v[240:241]\n"
"v_pk_mul_f32 v[36:37], v[36:37], v[240:241]\n"
"v_pk_mul_f32 v[38:39], v[38:39], v[240:241]\n"
"v_pk_mul_f32 v[40:41], v[40:41], v[240:241]\n"
"v_pk_mul_f32 v[42:43], v[42:43], v[240:241]\n"
"v_pk_mul_f32 v[44:45], v[44:45], v[240:241]\n"
"v_pk_mul_f32 v[46:47], v[46:47], v[240:241]\n"
"v_cvt_pk_bf16_f32 v32, v32, v33\n"
"v_cvt_pk_bf16_f32 v33, v34, v35\n"
"v_cvt_pk_bf16_f32 v34, v36, v37\n"
"v_cvt_pk_bf16_f32 v35, v38, v39\n"
"v_cvt_pk_bf16_f32 v36, v40, v41\n"
"v_cvt_pk_bf16_f32 v37, v42, v43\n"
"v_cvt_pk_bf16_f32 v38, v44, v45\n"
"v_cvt_pk_bf16_f32 v39, v46, v47\n"
"buffer_store_dwordx4 v[32:35], v228, s[28:31], s57 offen offset:0 sc1\n"
"buffer_store_dwordx4 v[36:39], v228, s[28:31], s57 offen offset:2048 sc1\n"
"v_accvgpr_read_b32 v48, a112\n"
"v_accvgpr_read_b32 v49, a113\n"
"v_accvgpr_read_b32 v50, a114\n"
"v_accvgpr_read_b32 v51, a115\n"
"v_accvgpr_read_b32 v52, a116\n"
"v_accvgpr_read_b32 v53, a117\n"
"v_accvgpr_read_b32 v54, a118\n"
"v_accvgpr_read_b32 v55, a119\n"
"v_accvgpr_read_b32 v56, a120\n"
"v_accvgpr_read_b32 v57, a121\n"
"v_accvgpr_read_b32 v58, a122\n"
"v_accvgpr_read_b32 v59, a123\n"
"v_accvgpr_read_b32 v60, a124\n"
"v_accvgpr_read_b32 v61, a125\n"
"v_accvgpr_read_b32 v62, a126\n"
"v_accvgpr_read_b32 v63, a127\n"
"v_pk_mul_f32 v[48:49], v[48:49], v[242:243]\n"
"v_pk_mul_f32 v[50:51], v[50:51], v[242:243]\n"
"v_pk_mul_f32 v[52:53], v[52:53], v[242:243]\n"
"v_pk_mul_f32 v[54:55], v[54:55], v[242:243]\n"
"v_pk_mul_f32 v[56:57], v[56:57], v[242:243]\n"
"v_pk_mul_f32 v[58:59], v[58:59], v[242:243]\n"
"v_pk_mul_f32 v[60:61], v[60:61], v[242:243]\n"
"v_pk_mul_f32 v[62:63], v[62:63], v[242:243]\n"
"v_cvt_pk_bf16_f32 v48, v48, v49\n"
"v_cvt_pk_bf16_f32 v49, v50, v51\n"
"v_cvt_pk_bf16_f32 v50, v52, v53\n"
"v_cvt_pk_bf16_f32 v51, v54, v55\n"
"v_cvt_pk_bf16_f32 v52, v56, v57\n"
"v_cvt_pk_bf16_f32 v53, v58, v59\n"
"v_cvt_pk_bf16_f32 v54, v60, v61\n"
"v_cvt_pk_bf16_f32 v55, v62, v63\n"
"buffer_store_dwordx4 v[48:51], v228, s[28:31], s57 offen offset:1024 sc1\n"
"buffer_store_dwordx4 v[52:55], v228, s[28:31], s57 offen offset:3072 sc1\n"
"s_waitcnt vmcnt(51)\n"
"ds_read_b64_tr_b16 v[128:129], v226 offset:34816\n"
"ds_read_b64_tr_b16 v[130:131], v226 offset:35840\n"
"ds_read_b64_tr_b16 v[132:133], v250 offset:34816\n"
"ds_read_b64_tr_b16 v[134:135], v250 offset:35840\n"
"s_waitcnt vmcnt(49)\n"
"ds_read_b64_tr_b16 v[136:137], v251 offset:2048\n"
"ds_read_b64_tr_b16 v[138:139], v251 offset:3072\n"
"ds_read_b64_tr_b16 v[140:141], v255 offset:2048\n"
"ds_read_b64_tr_b16 v[142:143], v255 offset:3072\n"
"s_waitcnt lgkmcnt(4)\n"
"s_add_u32 m0, s45, 100352\n"
"s_add_u32 s56, s49, 557056\n"
"s_and_b32 s56, s56, 0xfffff\n"
"buffer_load_dwordx4 v227, s[24:27], s56 offen lds\n"
"buffer_load_dwordx4 v252, s[24:27], s56 offen offset:1024 lds\n"
"s_waitcnt lgkmcnt(0)\n"
"s_add_u32 m0, s45, 133120\n"
"s_add_u32 s56, s49, 573440\n"
"s_and_b32 s56, s56, 0xfffff\n"
"buffer_load_dwordx4 v227, s[24:27], s56 offen lds\n"
"buffer_load_dwordx4 v252, s[24:27], s56 offen offset:1024 lds\n"
"s_waitcnt vmcnt(4)\n"
"s_barrier\n"
"s_mov_b64 exec, s[46:47]\n"
"buffer_store_dword v249, v246, s[40:43], s59 offen sc1\n"
"s_mov_b64 exec, -1\n"
"s_add_u32 m0, s45, 81920\n"
"s_add_u32 s56, s49, 589824\n"
"s_and_b32 s56, s56, 0xfffff\n"
"buffer_load_dwordx4 v227, s[24:27], s56 offen lds\n"
"buffer_load_dwordx4 v252, s[24:27], s56 offen offset:1024 lds\n"
"s_add_u32 m0, s45, 83968\n"
"s_add_u32 s56, s49, 606208\n"
"s_and_b32 s56, s56, 0xfffff\n"
"buffer_load_dwordx4 v227, s[24:27], s56 offen lds\n"
"buffer_load_dwordx4 v252, s[24:27], s56 offen offset:1024 lds\n"
"ds_read_b64_tr_b16 v[144:145], v224 offset:32768\n"
"ds_read_b64_tr_b16 v[146:147], v224 offset:33792\n"
"ds_read_b64_tr_b16 v[148:149], v225 offset:32768\n"
"ds_read_b64_tr_b16 v[150:151], v225 offset:33792\n"
"ds_read_b64_tr_b16 v[152:153], v224 offset:34816\n"
"ds_read_b64_tr_b16 v[154:155], v224 offset:35840\n"
"ds_read_b64_tr_b16 v[156:157], v225 offset:34816\n"
"ds_read_b64_tr_b16 v[158:159], v225 offset:35840\n"
"ds_read_b64_tr_b16 a[64:65], v251 offset:16384\n"
"ds_read_b64_tr_b16 a[66:67], v251 offset:17408\n"
"ds_read_b64_tr_b16 a[68:69], v255 offset:16384\n"
"ds_read_b64_tr_b16 a[70:71], v255 offset:17408\n"
"s_waitcnt lgkmcnt(8)\n"
"s_add_u32 m0, s45, 32768\n"
"s_add_u32 s56, s49, 622592\n"
"s_and_b32 s56, s56, 0xfffff\n"
"buffer_load_dwordx4 v227, s[24:27], s56 offen lds\n"
"buffer_load_dwordx4 v252, s[24:27], s56 offen offset:1024 lds\n"
"ds_read_b64_tr_b16 a[72:73], v251 offset:18432\n"
"ds_read_b64_tr_b16 a[74:75], v251 offset:19456\n"
"ds_read_b64_tr_b16 a[76:77], v255 offset:18432\n"
"ds_read_b64_tr_b16 a[78:79], v255 offset:19456\n"
"s_waitcnt lgkmcnt(8)\n"
"s_add_u32 m0, s45, 34816\n"
"s_add_u32 s56, s49, 638976\n"
"s_and_b32 s56, s56, 0xfffff\n"
"buffer_load_dwordx4 v227, s[24:27], s56 offen lds\n"
"buffer_load_dwordx4 v252, s[24:27], s56 offen offset:1024 lds\n"
"ds_read_b64_tr_b16 a[80:81], v224 offset:0\n"
"ds_read_b64_tr_b16 a[82:83], v224 offset:1024\n"
"ds_read_b64_tr_b16 a[84:85], v225 offset:0\n"
"ds_read_b64_tr_b16 a[86:87], v225 offset:1024\n"
"s_waitcnt lgkmcnt(8)\n"
"s_add_u32 m0, s45, 147456\n"
"s_add_u32 s56, s49, 655360\n"
"s_and_b32 s56, s56, 0xfffff\n"
"buffer_load_dwordx4 v227, s[24:27], s56 offen lds\n"
"buffer_load_dwordx4 v252, s[24:27], s56 offen offset:1024 lds\n"
"ds_read_b64_tr_b16 a[88:89], v224 offset:2048\n"
"ds_read_b64_tr_b16 a[90:91], v224 offset:3072\n"
"ds_read_b64_tr_b16 a[92:93], v225 offset:2048\n"
"ds_read_b64_tr_b16 a[94:95], v225 offset:3072\n"
"s_waitcnt lgkmcnt(8)\n"
"s_add_u32 m0, s45, 149504\n"
"s_add_u32 s56, s49, 671744\n"
"s_and_b32 s56, s56, 0xfffff\n"
"buffer_load_dwordx4 v227, s[24:27], s56 offen lds\n"
"buffer_load_dwordx4 v252, s[24:27], s56 offen offset:1024 lds\n"
"ds_read_b64_tr_b16 a[96:97], v226 offset:49152\n"
"ds_read_b64_tr_b16 a[98:99], v226 offset:50176\n"
"ds_read_b64_tr_b16 a[100:101], v250 offset:49152\n"
"ds_read_b64_tr_b16 a[102:103], v250 offset:50176\n"
"s_waitcnt lgkmcnt(8)\n"
"s_add_u32 m0, s45, 0\n"
"s_add_u32 s56, s49, 688128\n"
"s_and_b32 s56, s56, 0xfffff\n"
"buffer_load_dwordx4 v227, s[24:27], s56 offen lds\n"
"buffer_load_dwordx4 v252, s[24:27], s56 offen offset:1024 lds\n"
"ds_read_b64_tr_b16 a[104:105], v224 offset:16384\n"
"ds_read_b64_tr_b16 a[106:107], v224 offset:17408\n"
"ds_read_b64_tr_b16 a[108:109], v225 offset:16384\n"
"ds_read_b64_tr_b16 a[110:111], v225 offset:17408\n"
"s_waitcnt lgkmcnt(8)\n"
"s_add_u32 m0, s45, 2048\n"
"s_add_u32 s56, s49, 704512\n"
"s_and_b32 s56, s56, 0xfffff\n"
"buffer_load_dwordx4 v227, s[24:27], s56 offen lds\n"
"buffer_load_dwordx4 v252, s[24:27], s56 offen offset:1024 lds\n"
"ds_read_b64_tr_b16 a[112:113], v226 offset:51200\n"
"ds_read_b64_tr_b16 a[114:115], v226 offset:52224\n"
"ds_read_b64_tr_b16 a[116:117], v250 offset:51200\n"
"ds_read_b64_tr_b16 a[118:119], v250 offset:52224\n"
"s_waitcnt lgkmcnt(8)\n"
"s_add_u32 m0, s45, 114688\n"
"s_add_u32 s56, s49, 720896\n"
"s_and_b32 s56, s56, 0xfffff\n"
"buffer_load_dwordx4 v227, s[24:27], s56 offen lds\n"
"buffer_load_dwordx4 v252, s[24:27], s56 offen offset:1024 lds\n"
"ds_read_b64_tr_b16 a[120:121], v224 offset:49152\n"
"ds_read_b64_tr_b16 a[122:123], v224 offset:50176\n"
"ds_read_b64_tr_b16 a[124:125], v225 offset:49152\n"
"ds_read_b64_tr_b16 a[126:127], v225 offset:50176\n"
"s_waitcnt lgkmcnt(8)\n"
"s_add_u32 m0, s45, 16384\n"
"s_add_u32 s56, s49, 737280\n"
"s_and_b32 s56, s56, 0xfffff\n"
"buffer_load_dwordx4 v227, s[24:27], s56 offen lds\n"
"buffer_load_dwordx4 v252, s[24:27], s56 offen offset:1024 lds\n"
"ds_read_b64_tr_b16 a[128:129], v224 offset:18432\n"
"ds_read_b64_tr_b16 a[130:131], v224 offset:19456\n"
"ds_read_b64_tr_b16 a[132:133], v225 offset:18432\n"
"ds_read_b64_tr_b16 a[134:135], v225 offset:19456\n"
"s_waitcnt lgkmcnt(8)\n"
"s_add_u32 m0, s45, 116736\n"
"s_add_u32 s56, s49, 753664\n"
"s_and_b32 s56, s56, 0xfffff\n"
"buffer_load_dwordx4 v227, s[24:27], s56 offen lds\n"
"buffer_load_dwordx4 v252, s[24:27], s56 offen offset:1024 lds\n"
"ds_read_b64_tr_b16 a[136:137], v251 offset:0\n"
"ds_read_b64_tr_b16 a[138:139], v251 offset:1024\n"
"ds_read_b64_tr_b16 a[140:141], v255 offset:0\n"
"ds_read_b64_tr_b16 a[142:143], v255 offset:1024\n"
"s_waitcnt lgkmcnt(8)\n"
"s_add_u32 m0, s45, 49152\n"
"s_add_u32 s56, s49, 770048\n"
"s_and_b32 s56, s56, 0xfffff\n"
"buffer_load_dwordx4 v227, s[24:27], s56 offen lds\n"
"buffer_load_dwordx4 v252, s[24:27], s56 offen offset:1024 lds\n"
"ds_read_b64_tr_b16 a[144:145], v224 offset:51200\n"
"ds_read_b64_tr_b16 a[146:147], v224 offset:52224\n"
"ds_read_b64_tr_b16 a[148:149], v225 offset:51200\n"
"ds_read_b64_tr_b16 a[150:151], v225 offset:52224\n"
"s_waitcnt lgkmcnt(8)\n"
"s_add_u32 m0, s45, 18432\n"
"s_add_u32 s56, s49, 786432\n"
"s_and_b32 s56, s56, 0xfffff\n"
"buffer_load_dwordx4 v227, s[24:27], s56 offen lds\n"
"buffer_load_dwordx4 v252, s[24:27], s56 offen offset:1024 lds\n"
"ds_read_b64_tr_b16 a[152:153], v226 offset:32768\n"
"ds_read_b64_tr_b16 a[154:155], v226 offset:33792\n"
"ds_read_b64_tr_b16 a[156:157], v250 offset:32768\n"
"ds_read_b64_tr_b16 a[158:159], v250 offset:33792\n"
"s_waitcnt lgkmcnt(8)\n"
"s_add_u32 m0, s45, 131072\n"
"s_add_u32 s56, s49, 802816\n"
"s_and_b32 s56, s56, 0xfffff\n"
"buffer_load_dwordx4 v227, s[24:27], s56 offen lds\n"
"buffer_load_dwordx4 v252, s[24:27], s56 offen offset:1024 lds\n"
"ds_read_b64_tr_b16 a[160:161], v226 offset:0\n"
"ds_read_b64_tr_b16 a[162:163], v226 offset:1024\n"
"ds_read_b64_tr_b16 a[164:165], v250 offset:0\n"
"ds_read_b64_tr_b16 a[166:167], v250 offset:1024\n"
"s_waitcnt lgkmcnt(8)\n"
"s_add_u32 m0, s45, 51200\n"
"s_add_u32 s56, s49, 819200\n"
"s_and_b32 s56, s56, 0xfffff\n"
"buffer_load_dwordx4 v227, s[24:27], s56 offen lds\n"
"buffer_load_dwordx4 v252, s[24:27], s56 offen offset:1024 lds\n"
"ds_read_b64_tr_b16 a[168:169], v226 offset:2048\n"
"ds_read_b64_tr_b16 a[170:171], v226 offset:3072\n"
"ds_read_b64_tr_b16 a[172:173], v250 offset:2048\n"
"ds_read_b64_tr_b16 a[174:175], v250 offset:3072\n"
"s_waitcnt lgkmcnt(8)\n"
"s_add_u32 m0, s45, 98304\n"
"s_add_u32 s56, s49, 835584\n"
"s_and_b32 s56, s56, 0xfffff\n"
"buffer_load_dwordx4 v227, s[24:27], s56 offen lds\n"
"buffer_load_dwordx4 v252, s[24:27], s56 offen offset:1024 lds\n"
"s_waitcnt vmcnt(35)\n"
"ds_read_b64_tr_b16 a[176:177], v226 offset:34816\n"
"ds_read_b64_tr_b16 a[178:179], v226 offset:35840\n"
"ds_read_b64_tr_b16 a[180:181], v250 offset:34816\n"
"ds_read_b64_tr_b16 a[182:183], v250 offset:35840\n"
"s_waitcnt lgkmcnt(8)\n"
"s_add_u32 m0, s45, 65536\n"
"s_add_u32 s56, s49, 851968\n"
"s_and_b32 s56, s56, 0xfffff\n"
"buffer_load_dwordx4 v227, s[24:27], s56 offen lds\n"
"buffer_load_dwordx4 v252, s[24:27], s56 offen offset:1024 lds\n"
"s_waitcnt vmcnt(35)\n"
"ds_read_b64_tr_b16 a[184:185], v251 offset:2048\n"
"ds_read_b64_tr_b16 a[186:187], v251 offset:3072\n"
"ds_read_b64_tr_b16 a[188:189], v255 offset:2048\n"
"ds_read_b64_tr_b16 a[190:191], v255 offset:3072\n"
"s_waitcnt lgkmcnt(8)\n"
"s_add_u32 m0, s45, 67584\n"
"s_add_u32 s56, s49, 868352\n"
"s_and_b32 s56, s56, 0xfffff\n"
"buffer_load_dwordx4 v227, s[24:27], s56 offen lds\n"
"buffer_load_dwordx4 v252, s[24:27], s56 offen offset:1024 lds\n"
"s_waitcnt vmcnt(34)\n"
"ds_read_b64_tr_b16 v[64:65], v226 offset:16384\n"
"ds_read_b64_tr_b16 v[66:67], v226 offset:17408\n"
"ds_read_b64_tr_b16 v[68:69], v250 offset:16384\n"
"ds_read_b64_tr_b16 v[70:71], v250 offset:17408\n"
"s_waitcnt lgkmcnt(8)\n"
"s_add_u32 m0, s45, 100352\n"
"s_add_u32 s56, s49, 884736\n"
"s_and_b32 s56, s56, 0xfffff\n"
"buffer_load_dwordx4 v227, s[24:27], s56 offen lds\n"
"buffer_load_dwordx4 v252, s[24:27], s56 offen offset:1024 lds\n"
"s_waitcnt vmcnt(34)\n"
"ds_read_b64_tr_b16 v[72:73], v226 offset:18432\n"
"ds_read_b64_tr_b16 v[74:75], v226 offset:19456\n"
"ds_read_b64_tr_b16 v[76:77], v250 offset:18432\n"
"ds_read_b64_tr_b16 v[78:79], v250 offset:19456\n"
"s_waitcnt lgkmcnt(8)\n"
"s_add_u32 m0, s45, 133120\n"
"s_add_u32 s56, s49, 901120\n"
"s_and_b32 s56, s56, 0xfffff\n"
"buffer_load_dwordx4 v227, s[24:27], s56 offen lds\n"
"buffer_load_dwordx4 v252, s[24:27], s56 offen offset:1024 lds\n"
"s_waitcnt lgkmcnt(4)\n"
"s_add_u32 m0, s45, 81920\n"
"s_add_u32 s56, s49, 917504\n"
"s_and_b32 s56, s56, 0xfffff\n"
"buffer_load_dwordx4 v227, s[24:27], s56 offen lds\n"
"buffer_load_dwordx4 v252, s[24:27], s56 offen offset:1024 lds\n"
"s_waitcnt lgkmcnt(0)\n"
"s_add_u32 m0, s45, 83968\n"
"s_add_u32 s56, s49, 933888\n"
"s_and_b32 s56, s56, 0xfffff\n"
"buffer_load_dwordx4 v227, s[24:27], s56 offen lds\n"
"buffer_load_dwordx4 v252, s[24:27], s56 offen offset:1024 lds\n"
"s_mov_b64 exec, s[46:47]\n"
"buffer_load_dword v248, v245, s[40:43], s59 offen sc1\n"
"s_mov_b64 exec, -1\n"
"s_waitcnt vmcnt(39)\n"
"ds_read_b64_tr_b16 v[80:81], v224 offset:32768\n"
"ds_read_b64_tr_b16 v[82:83], v224 offset:33792\n"
"ds_read_b64_tr_b16 v[84:85], v225 offset:32768\n"
"ds_read_b64_tr_b16 v[86:87], v225 offset:33792\n"
"s_waitcnt vmcnt(37)\n"
"ds_read_b64_tr_b16 v[88:89], v224 offset:34816\n"
"ds_read_b64_tr_b16 v[90:91], v224 offset:35840\n"
"ds_read_b64_tr_b16 v[92:93], v225 offset:34816\n"
"ds_read_b64_tr_b16 v[94:95], v225 offset:35840\n"
"s_waitcnt vmcnt(35)\n"
"ds_read_b64_tr_b16 v[96:97], v251 offset:16384\n"
"ds_read_b64_tr_b16 v[98:99], v251 offset:17408\n"
"ds_read_b64_tr_b16 v[100:101], v255 offset:16384\n"
"ds_read_b64_tr_b16 v[102:103], v255 offset:17408\n"
"s_waitcnt lgkmcnt(8)\n"
"s_add_u32 m0, s45, 32768\n"
"s_add_u32 s56, s49, 950272\n"
"s_and_b32 s56, s56, 0xfffff\n"
"buffer_load_dwordx4 v227, s[24:27], s56 offen lds\n"
"buffer_load_dwordx4 v252, s[24:27], s56 offen offset:1024 lds\n"
"s_waitcnt vmcnt(35)\n"
"ds_read_b64_tr_b16 v[104:105], v251 offset:18432\n"
"ds_read_b64_tr_b16 v[106:107], v251 offset:19456\n"
"ds_read_b64_tr_b16 v[108:109], v255 offset:18432\n"
"ds_read_b64_tr_b16 v[110:111], v255 offset:19456\n"
"s_waitcnt lgkmcnt(8)\n"
"s_add_u32 m0, s45, 34816\n"
"s_add_u32 s56, s49, 966656\n"
"s_and_b32 s56, s56, 0xfffff\n"
"buffer_load_dwordx4 v227, s[24:27], s56 offen lds\n"
"buffer_load_dwordx4 v252, s[24:27], s56 offen offset:1024 lds\n"
"s_waitcnt vmcnt(35)\n"
"ds_read_b64_tr_b16 v[112:113], v224 offset:0\n"
"ds_read_b64_tr_b16 v[114:115], v224 offset:1024\n"
"ds_read_b64_tr_b16 v[116:117], v225 offset:0\n"
"ds_read_b64_tr_b16 v[118:119], v225 offset:1024\n"
"s_waitcnt lgkmcnt(8)\n"
"s_add_u32 m0, s45, 147456\n"
"s_add_u32 s56, s49, 983040\n"
"s_and_b32 s56, s56, 0xfffff\n"
"buffer_load_dwordx4 v227, s[24:27], s56 offen lds\n"
"buffer_load_dwordx4 v252, s[24:27], s56 offen offset:1024 lds\n"
"s_waitcnt vmcnt(35)\n"
"ds_read_b64_tr_b16 v[120:121], v224 offset:2048\n"
"ds_read_b64_tr_b16 v[122:123], v224 offset:3072\n"
"ds_read_b64_tr_b16 v[124:125], v225 offset:2048\n"
"ds_read_b64_tr_b16 v[126:127], v225 offset:3072\n"
"s_waitcnt lgkmcnt(8)\n"
"s_add_u32 m0, s45, 149504\n"
"s_add_u32 s56, s49, 999424\n"
"s_and_b32 s56, s56, 0xfffff\n"
"buffer_load_dwordx4 v227, s[24:27], s56 offen lds\n"
"buffer_load_dwordx4 v252, s[24:27], s56 offen offset:1024 lds\n"
"s_waitcnt lgkmcnt(4)\n"
"s_add_u32 m0, s45, 0\n"
"s_add_u32 s56, s49, 1015808\n"
"s_and_b32 s56, s56, 0xfffff\n"
"buffer_load_dwordx4 v227, s[24:27], s56 offen lds\n"
"buffer_load_dwordx4 v252, s[24:27], s56 offen offset:1024 lds\n"
"s_waitcnt lgkmcnt(0)\n"
"s_add_u32 m0, s45, 2048\n"
"s_add_u32 s56, s49, 1032192\n"
"s_and_b32 s56, s56, 0xfffff\n"
"buffer_load_dwordx4 v227, s[24:27], s56 offen lds\n"
"buffer_load_dwordx4 v252, s[24:27], s56 offen offset:1024 lds\n"
"s_waitcnt vmcnt(12)\n"
"v_cmp_ne_u32 vcc, v248, v247\n"
"s_cmp_lg_u32 s44, 0\n"
"s_cbranch_scc1 Lpd_%=\n"
"s_cbranch_vccz Lpd_%=\n"
"s_mov_b32 s60, 0\n"
"Lpoll_%=:\n"
"buffer_load_dword v248, v245, s[40:43], s59 offen sc1\n"
"buffer_load_dword v249, v245, s[40:43], 0 offen sc1\n"
"s_waitcnt vmcnt(0)\n"
"v_cmp_ne_u32 vcc, v248, v247\n"
"s_nop 1\n"
"s_cbranch_vccz Lpd_%=\n"
"buffer_store_dword v249, v246, s[40:43], s59 offen sc1\n"
"s_add_u32 s60, s60, 1\n"
"s_cmp_lt_u32 s60, 400000\n"
"s_sleep 1\n"
"s_cbranch_scc1 Lpoll_%=\n"
"Lpd_%=:\n"
"s_barrier\n"
"s_add_u32 s57, s48, 0\n"
"s_and_b32 s57, s57, 0x1ffff\n"
"buffer_load_dwordx4 v[0:3], v228, s[32:35], s57 offen offset:0 sc1\n"
"buffer_load_dwordx4 v[4:7], v228, s[32:35], s57 offen offset:1024 sc1\n"
"buffer_load_dwordx4 v[8:11], v228, s[32:35], s57 offen offset:2048 sc1\n"
"buffer_load_dwordx4 v[12:15], v228, s[32:35], s57 offen offset:3072 sc1\n"
"s_add_u32 s57, s48, 90112\n"
"s_and_b32 s57, s57, 0x1ffff\n"
"buffer_load_dwordx4 v[16:19], v228, s[32:35], s57 offen offset:0 sc1\n"
"buffer_load_dwordx4 v[20:23], v228, s[32:35], s57 offen offset:1024 sc1\n"
"buffer_load_dwordx4 v[24:27], v228, s[32:35], s57 offen offset:2048 sc1\n"
"buffer_load_dwordx4 v[28:31], v228, s[32:35], s57 offen offset:3072 sc1\n"
"s_add_u32 s57, s48, 4096\n"
"s_and_b32 s57, s57, 0x1ffff\n"
"buffer_load_dwordx4 v[32:35], v228, s[32:35], s57 offen offset:0 sc1\n"
"buffer_load_dwordx4 v[36:39], v228, s[32:35], s57 offen offset:1024 sc1\n"
"ds_read_b64_tr_b16 v[40:41], v226 offset:49152\n"
"ds_read_b64_tr_b16 v[42:43], v226 offset:50176\n"
"ds_read_b64_tr_b16 v[44:45], v250 offset:49152\n"
"ds_read_b64_tr_b16 v[46:47], v250 offset:50176\n"
"ds_read_b64_tr_b16 v[48:49], v224 offset:16384\n"
"ds_read_b64_tr_b16 v[50:51], v224 offset:17408\n"
"ds_read_b64_tr_b16 v[52:53], v225 offset:16384\n"
"ds_read_b64_tr_b16 v[54:55], v225 offset:17408\n"
"s_waitcnt vmcnt(9)\n"
"v_mfma_f32_32x32x16_bf16 a[0:15], a[192:195], v[0:3], 0\n"
"s_mov_b64 exec, s[46:47]\n"
"buffer_load_dword v249, v245, s[40:43], 0 offen sc1\n"
"s_mov_b64 exec, -1\n"
"v_mfma_f32_32x32x16_bf16 a[32:47], a[196:199], v[0:3], 0\n"
"s_waitcnt vmcnt(9)\n"
"v_mfma_f32_32x32x16_bf16 a[16:31], a[192:195], v[4:7], 0\n"
"v_mfma_f32_32x32x16_bf16 a[48:63], a[196:199], v[4:7], 0\n"
"s_waitcnt vmcnt(8)\n"
"v_mfma_f32_32x32x16_bf16 a[0:15], a[200:203], v[8:11], a[0:15]\n"
"v_mfma_f32_32x32x16_bf16 a[32:47], a[204:207], v[8:11], a[32:47]\n"
"s_waitcnt vmcnt(7)\n"
"v_mfma_f32_32x32x16_bf16 a[16:31], a[200:203], v[12:15], a[16:31]\n"
"buffer_load_dwordx4 v[56:59], v228, s[32:35], s57 offen offset:2048 sc1\n"
"buffer_load_dwordx4 v[60:63], v228, s[32:35], s57 offen offset:3072 sc1\n"
"v_mfma_f32_32x32x16_bf16 a[48:63], a[204:207], v[12:15], a[48:63]\n"
"s_waitcnt lgkmcnt(6)\n"
"s_waitcnt vmcnt(8)\n"
"v_mfma_f32_32x32x16_bf16 a[0:15], v[40:43], v[16:19], a[0:15]\n"
"s_waitcnt lgkmcnt(3)\n"
"v_mfma_f32_32x32x16_bf16 a[32:47], v[44:47], v[16:19], a[32:47]\n"
"ds_read_b64_tr_b16 a[200:201], v226 offset:51200\n"
"ds_read_b64_tr_b16 a[202:203], v226 offset:52224\n"
"ds_read_b64_tr_b16 a[204:205], v250 offset:51200\n"
"ds_read_b64_tr_b16 a[206:207], v250 offset:52224\n"
"s_waitcnt vmcnt(7)\n"
"v_mfma_f32_32x32x16_bf16 a[16:31], v[40:43], v[20:23], a[16:31]\n"
"s_add_u32 s57, s48, 94208\n"
"s_and_b32 s57, s57, 0x1ffff\n"
"buffer_load_dwordx4 a[192:195], v228, s[32:35], s57 offen offset:0 sc1\n"
"buffer_load_dwordx4 a[196:199], v228, s[32:35], s57 offen offset:1024 sc1\n"
"v_mfma_f32_32x32x16_bf16 a[48:63], v[44:47], v[20:23], a[48:63]\n"
"buffer_load_dwordx4 v[0:3], v228, s[32:35], s57 offen offset:2048 sc1\n"
"buffer_load_dwordx4 v[4:7], v228, s[32:35], s57 offen offset:3072 sc1\n"
"s_waitcnt lgkmcnt(4)\n"
"s_waitcnt vmcnt(10)\n"
"v_mfma_f32_32x32x16_bf16 a[0:15], v[48:51], v[24:27], a[0:15]\n"
"v_mfma_f32_32x32x16_bf16 a[32:47], v[52:55], v[24:27], a[32:47]\n"
"ds_read_b64_tr_b16 v[40:41], v224 offset:49152\n"
"ds_read_b64_tr_b16 v[42:43], v224 offset:50176\n"
"ds_read_b64_tr_b16 v[44:45], v225 offset:49152\n"
"ds_read_b64_tr_b16 v[46:47], v225 offset:50176\n"
"s_waitcnt vmcnt(9)\n"
"v_mfma_f32_32x32x16_bf16 a[16:31], v[48:51], v[28:31], a[16:31]\n"
"s_add_u32 s57, s48, 8192\n"
"s_and_b32 s57, s57, 0x1ffff\n"
"buffer_load_dwordx4 v[8:11], v228, s[32:35], s57 offen offset:0 sc1\n"
"buffer_load_dwordx4 v[12:15], v228, s[32:35], s57 offen offset:1024 sc1\n"
"v_mfma_f32_32x32x16_bf16 a[48:63], v[52:55], v[28:31], a[48:63]\n"
"s_waitcnt vmcnt(10)\n"
"v_mfma_f32_32x32x16_bf16 a[0:15], a[208:211], v[32:35], a[0:15]\n"
"v_mfma_f32_32x32x16_bf16 a[32:47], a[212:215], v[32:35], a[32:47]\n"
"s_waitcnt vmcnt(9)\n"
"v_mfma_f32_32x32x16_bf16 a[16:31], a[208:211], v[36:39], a[16:31]\n"
"buffer_load_dwordx4 v[16:19], v228, s[32:35], s57 offen offset:2048 sc1\n"
"buffer_load_dwordx4 v[20:23], v228, s[32:35], s57 offen offset:3072 sc1\n"
"v_mfma_f32_32x32x16_bf16 a[48:63], a[212:215], v[36:39], a[48:63]\n"
"s_waitcnt vmcnt(9)\n"
"v_mfma_f32_32x32x16_bf16 a[0:15], a[216:219], v[56:59], a[0:15]\n"
"v_mfma_f32_32x32x16_bf16 a[32:47], a[220:223], v[56:59], a[32:47]\n"
"s_waitcnt vmcnt(8)\n"
"v_mfma_f32_32x32x16_bf16 a[16:31], a[216:219], v[60:63], a[16:31]\n"
"s_add_u32 s57, s48, 98304\n"
"s_and_b32 s57, s57, 0x1ffff\n"
"buffer_load_dwordx4 v[48:51], v228, s[32:35], s57 offen offset:0 sc1\n"
"buffer_load_dwordx4 v[52:55], v228, s[32:35], s57 offen offset:1024 sc1\n"
"v_mfma_f32_32x32x16_bf16 a[48:63], a[220:223], v[60:63], a[48:63]\n"
"buffer_load_dwordx4 v[24:27], v228, s[32:35], s57 offen offset:2048 sc1\n"
"buffer_load_dwordx4 v[28:31], v228, s[32:35], s57 offen offset:3072 sc1\n"
"s_waitcnt lgkmcnt(0)\n"
"s_waitcnt vmcnt(11)\n"
"v_mfma_f32_32x32x16_bf16 a[0:15], a[200:203], a[192:195], a[0:15]\n"
"v_mfma_f32_32x32x16_bf16 a[32:47], a[204:207], a[192:195], a[32:47]\n"
"ds_read_b64_tr_b16 a[216:217], v224 offset:18432\n"
"ds_read_b64_tr_b16 a[218:219], v224 offset:19456\n"
"ds_read_b64_tr_b16 a[220:221], v225 offset:18432\n"
"ds_read_b64_tr_b16 a[222:223], v225 offset:19456\n"
"s_waitcnt vmcnt(10)\n"
"v_mfma_f32_32x32x16_bf16 a[16:31], a[200:203], a[196:199], a[16:31]\n"
"s_add_u32 s57, s48, 12288\n"
"s_and_b32 s57, s57, 0x1ffff\n"
"buffer_load_dwordx4 a[208:211], v228, s[32:35], s57 offen offset:0 sc1\n"
"buffer_load_dwordx4 a[212:215], v228, s[32:35], s57 offen offset:1024 sc1\n"
"v_mfma_f32_32x32x16_bf16 a[48:63], a[204:207], a[196:199], a[48:63]\n"
"buffer_load_dwordx4 v[32:35], v228, s[32:35], s57 offen offset:2048 sc1\n"
"buffer_load_dwordx4 v[36:39], v228, s[32:35], s57 offen offset:3072 sc1\n"
"s_waitcnt vmcnt(13)\n"
"v_mfma_f32_32x32x16_bf16 a[0:15], v[40:43], v[0:3], a[0:15]\n"
"v_mfma_f32_32x32x16_bf16 a[32:47], v[44:47], v[0:3], a[32:47]\n"
"ds_read_b64_tr_b16 a[200:201], v251 offset:0\n"
"ds_read_b64_tr_b16 a[202:203], v251 offset:1024\n"
"ds_read_b64_tr_b16 a[204:205], v255 offset:0\n"
"ds_read_b64_tr_b16 a[206:207], v255 offset:1024\n"
"s_waitcnt vmcnt(12)\n"
"v_mfma_f32_32x32x16_bf16 a[16:31], v[40:43], v[4:7], a[16:31]\n"
"s_add_u32 s57, s48, 102400\n"
"s_and_b32 s57, s57, 0x1ffff\n"
"buffer_load_dwordx4 v[56:59], v228, s[32:35], s57 offen offset:0 sc1\n"
"buffer_load_dwordx4 v[60:63], v228, s[32:35], s57 offen offset:1024 sc1\n"
"v_mfma_f32_32x32x16_bf16 a[48:63], v[44:47], v[4:7], a[48:63]\n"
"s_waitcnt vmcnt(13)\n"
"v_mfma_f32_32x32x16_bf16 a[0:15], a[224:227], v[8:11], a[0:15]\n"
"v_mfma_f32_32x32x16_bf16 a[32:47], a[228:231], v[8:11], a[32:47]\n"
"s_waitcnt vmcnt(12)\n"
"v_mfma_f32_32x32x16_bf16 a[16:31], a[224:227], v[12:15], a[16:31]\n"
"buffer_load_dwordx4 a[192:195], v228, s[32:35], s57 offen offset:2048 sc1\n"
"buffer_load_dwordx4 a[196:199], v228, s[32:35], s57 offen offset:3072 sc1\n"
"v_mfma_f32_32x32x16_bf16 a[48:63], a[228:231], v[12:15], a[48:63]\n"
"s_waitcnt vmcnt(13)\n"
"v_mfma_f32_32x32x16_bf16 a[0:15], a[232:235], v[16:19], a[0:15]\n"
"v_mfma_f32_32x32x16_bf16 a[32:47], a[236:239], v[16:19], a[32:47]\n"
"s_waitcnt vmcnt(12)\n"
"v_mfma_f32_32x32x16_bf16 a[16:31], a[232:235], v[20:23], a[16:31]\n"
"s_add_u32 s57, s48, 16384\n"
"s_and_b32 s57, s57, 0x1ffff\n"
"buffer_load_dwordx4 v[40:43], v228, s[32:35], s57 offen offset:0 sc1\n"
"buffer_load_dwordx4 v[44:47], v228, s[32:35], s57 offen offset:1024 sc1\n"
"v_mfma_f32_32x32x16_bf16 a[48:63], a[236:239], v[20:23], a[48:63]\n"
"buffer_load_dwordx4 v[0:3], v228, s[32:35], s57 offen offset:2048 sc1\n"
"buffer_load_dwordx4 v[4:7], v228, s[32:35], s57 offen offset:3072 sc1\n"
"s_waitcnt lgkmcnt(0)\n"
"s_waitcnt vmcnt(15)\n"
"v_mfma_f32_32x32x16_bf16 a[0:15], a[216:219], v[48:51], a[0:15]\n"
"s_mov_b64 exec, s[46:47]\n"
"buffer_store_dword v249, v246, s[40:43], s59 offen sc1\n"
"s_mov_b64 exec, -1\n"
"v_mfma_f32_32x32x16_bf16 a[32:47], a[220:223], v[48:51], a[32:47]\n"
"ds_read_b64_tr_b16 a[232:233], v224 offset:51200\n"
"ds_read_b64_tr_b16 a[234:235], v224 offset:52224\n"
"ds_read_b64_tr_b16 a[236:237], v225 offset:51200\n"
"ds_read_b64_tr_b16 a[238:239], v225 offset:52224\n"
"s_waitcnt vmcnt(15)\n"
"v_mfma_f32_32x32x16_bf16 a[16:31], a[216:219], v[52:55], a[16:31]\n"
"s_add_u32 s57, s48, 106496\n"
"s_and_b32 s57, s57, 0x1ffff\n"
"buffer_load_dwordx4 a[224:227], v228, s[32:35], s57 offen offset:0 sc1\n"
"buffer_load_dwordx4 a[228:231], v228, s[32:35], s57 offen offset:1024 sc1\n"
"v_mfma_f32_32x32x16_bf16 a[48:63], a[220:223], v[52:55], a[48:63]\n"
"buffer_load_dwordx4 v[8:11], v228, s[32:35], s57 offen offset:2048 sc1\n"
"buffer_load_dwordx4 v[12:15], v228, s[32:35], s57 offen offset:3072 sc1\n"
"s_waitcnt vmcnt(18)\n"
"v_mfma_f32_32x32x16_bf16 a[0:15], a[200:203], v[24:27], a[0:15]\n"
"v_mfma_f32_32x32x16_bf16 a[32:47], a[204:207], v[24:27], a[32:47]\n"
"ds_read_b64_tr_b16 v[16:17], v226 offset:32768\n"
"ds_read_b64_tr_b16 v[18:19], v226 offset:33792\n"
"ds_read_b64_tr_b16 v[20:21], v250 offset:32768\n"
"ds_read_b64_tr_b16 v[22:23], v250 offset:33792\n"
"s_waitcnt vmcnt(17)\n"
"v_mfma_f32_32x32x16_bf16 a[16:31], a[200:203], v[28:31], a[16:31]\n"
"v_mfma_f32_32x32x16_bf16 a[48:63], a[204:207], v[28:31], a[48:63]\n"
"s_waitcnt vmcnt(16)\n"
"v_mfma_f32_32x32x16_bf16 a[0:15], v[184:187], a[208:211], a[0:15]\n"
"v_mfma_f32_32x32x16_bf16 a[32:47], v[188:191], a[208:211], a[32:47]\n"
"s_waitcnt vmcnt(15)\n"
"v_mfma_f32_32x32x16_bf16 a[16:31], v[184:187], a[212:215], a[16:31]\n"
"s_add_u32 s57, s48, 20480\n"
"s_and_b32 s57, s57, 0x1ffff\n"
"buffer_load_dwordx4 a[216:219], v228, s[32:35], s57 offen offset:0 sc1\n"
"buffer_load_dwordx4 a[220:223], v228, s[32:35], s57 offen offset:1024 sc1\n"
"v_mfma_f32_32x32x16_bf16 a[48:63], v[188:191], a[212:215], a[48:63]\n"
"buffer_load_dwordx4 v[48:51], v228, s[32:35], s57 offen offset:2048 sc1\n"
"buffer_load_dwordx4 v[52:55], v228, s[32:35], s57 offen offset:3072 sc1\n"
"s_waitcnt vmcnt(18)\n"
"v_mfma_f32_32x32x16_bf16 a[0:15], v[200:203], v[32:35], a[0:15]\n"
"v_mfma_f32_32x32x16_bf16 a[32:47], v[204:207], v[32:35], a[32:47]\n"
"s_waitcnt vmcnt(17)\n"
"v_mfma_f32_32x32x16_bf16 a[16:31], v[200:203], v[36:39], a[16:31]\n"
"v_mfma_f32_32x32x16_bf16 a[48:63], v[204:207], v[36:39], a[48:63]\n"
"s_waitcnt lgkmcnt(2)\n"
"s_waitcnt vmcnt(16)\n"
"v_mfma_f32_32x32x16_bf16 a[0:15], a[232:235], v[56:59], a[0:15]\n"
"v_mfma_f32_32x32x16_bf16 a[32:47], a[236:239], v[56:59], a[32:47]\n"
"ds_read_b64_tr_b16 v[184:185], v226 offset:0\n"
"ds_read_b64_tr_b16 v[186:187], v226 offset:1024\n"
"ds_read_b64_tr_b16 v[188:189], v250 offset:0\n"
"ds_read_b64_tr_b16 v[190:191], v250 offset:1024\n"
"s_waitcnt vmcnt(15)\n"
"v_mfma_f32_32x32x16_bf16 a[16:31], a[232:235], v[60:63], a[16:31]\n"
"s_add_u32 s57, s48, 110592\n"
"s_and_b32 s57, s57, 0x1ffff\n"
"buffer_load_dwordx4 a[200:203], v228, s[32:35], s57 offen offset:0 sc1\n"
"buffer_load_dwordx4 a[204:207], v228, s[32:35], s57 offen offset:1024 sc1\n"
"v_mfma_f32_32x32x16_bf16 a[48:63], a[236:239], v[60:63], a[48:63]\n"
"buffer_load_dwordx4 v[24:27], v228, s[32:35], s57 offen offset:2048 sc1\n"
"buffer_load_dwordx4 v[28:31], v228, s[32:35], s57 offen offset:3072 sc1\n"
"s_waitcnt vmcnt(18)\n"
"v_mfma_f32_32x32x16_bf16 a[0:15], v[16:19], a[192:195], a[0:15]\n"
"s_waitcnt lgkmcnt(4)\n"
"v_mfma_f32_32x32x16_bf16 a[32:47], v[20:23], a[192:195], a[32:47]\n"
"ds_read_b64_tr_b16 a[208:209], v226 offset:2048\n"
"ds_read_b64_tr_b16 a[210:211], v226 offset:3072\n"
"ds_read_b64_tr_b16 a[212:213], v250 offset:2048\n"
"ds_read_b64_tr_b16 a[214:215], v250 offset:3072\n"
"s_waitcnt vmcnt(17)\n"
"v_mfma_f32_32x32x16_bf16 a[16:31], v[16:19], a[196:199], a[16:31]\n"
"v_mfma_f32_32x32x16_bf16 a[48:63], v[20:23], a[196:199], a[48:63]\n"
"s_waitcnt vmcnt(16)\n"
"v_mfma_f32_32x32x16_bf16 a[0:15], v[208:211], v[40:43], a[0:15]\n"
"v_mfma_f32_32x32x16_bf16 a[32:47], v[212:215], v[40:43], a[32:47]\n"
"s_waitcnt vmcnt(15)\n"
"v_mfma_f32_32x32x16_bf16 a[16:31], v[208:211], v[44:47], a[16:31]\n"
"s_add_u32 s57, s48, 24576\n"
"s_and_b32 s57, s57, 0x1ffff\n"
"buffer_load_dwordx4 v[200:203], v228, s[32:35], s57 offen offset:0 sc1\n"
"buffer_load_dwordx4 v[204:207], v228, s[32:35], s57 offen offset:1024 sc1\n"
"v_mfma_f32_32x32x16_bf16 a[48:63], v[212:215], v[44:47], a[48:63]\n"
"buffer_load_dwordx4 v[32:35], v228, s[32:35], s57 offen offset:2048 sc1\n"
"buffer_load_dwordx4 v[36:39], v228, s[32:35], s57 offen offset:3072 sc1\n"
"s_waitcnt vmcnt(18)\n"
"v_mfma_f32_32x32x16_bf16 a[0:15], v[136:139], v[0:3], a[0:15]\n"
"v_mfma_f32_32x32x16_bf16 a[32:47], v[140:143], v[0:3], a[32:47]\n"
"s_waitcnt vmcnt(17)\n"
"v_mfma_f32_32x32x16_bf16 a[16:31], v[136:139], v[4:7], a[16:31]\n"
"v_mfma_f32_32x32x16_bf16 a[48:63], v[140:143], v[4:7], a[48:63]\n"
"s_waitcnt lgkmcnt(2)\n"
"s_waitcnt vmcnt(15)\n"
"v_mfma_f32_32x32x16_bf16 a[0:15], v[184:187], a[224:227], a[0:15]\n"
"v_mfma_f32_32x32x16_bf16 a[32:47], v[188:191], a[224:227], a[32:47]\n"
"ds_read_b64_tr_b16 v[16:17], v226 offset:34816\n"
"ds_read_b64_tr_b16 v[18:19], v226 offset:35840\n"
"ds_read_b64_tr_b16 v[20:21], v250 offset:34816\n"
"ds_read_b64_tr_b16 v[22:23], v250 offset:35840\n"
"s_waitcnt vmcnt(14)\n"
"v_mfma_f32_32x32x16_bf16 a[16:31], v[184:187], a[228:231], a[16:31]\n"
"s_add_u32 s57, s48, 114688\n"
"s_and_b32 s57, s57, 0x1ffff\n"
"buffer_load_dwordx4 a[232:235], v228, s[32:35], s57 offen offset:0 sc1\n"
"buffer_load_dwordx4 a[236:239], v228, s[32:35], s57 offen offset:1024 sc1\n"
"v_mfma_f32_32x32x16_bf16 a[48:63], v[188:191], a[228:231], a[48:63]\n"
"buffer_load_dwordx4 v[56:59], v228, s[32:35], s57 offen offset:2048 sc1\n"
"buffer_load_dwordx4 v[60:63], v228, s[32:35], s57 offen offset:3072 sc1\n"
"s_waitcnt vmcnt(17)\n"
"v_mfma_f32_32x32x16_bf16 a[0:15], a[208:211], v[8:11], a[0:15]\n"
"s_waitcnt lgkmcnt(4)\n"
"v_mfma_f32_32x32x16_bf16 a[32:47], a[212:215], v[8:11], a[32:47]\n"
"ds_read_b64_tr_b16 a[192:193], v251 offset:2048\n"
"ds_read_b64_tr_b16 a[194:195], v251 offset:3072\n"
"ds_read_b64_tr_b16 a[196:197], v255 offset:2048\n"
"ds_read_b64_tr_b16 a[198:199], v255 offset:3072\n"
"s_waitcnt vmcnt(16)\n"
"v_mfma_f32_32x32x16_bf16 a[16:31], a[208:211], v[12:15], a[16:31]\n"
"v_mfma_f32_32x32x16_bf16 a[48:63], a[212:215], v[12:15], a[48:63]\n"
"s_waitcnt vmcnt(15)\n"
"v_mfma_f32_32x32x16_bf16 a[0:15], v[144:147], a[216:219], a[0:15]\n"
"v_mfma_f32_32x32x16_bf16 a[32:47], v[148:151], a[216:219], a[32:47]\n"
"s_waitcnt vmcnt(14)\n"
"v_mfma_f32_32x32x16_bf16 a[16:31], v[144:147], a[220:223], a[16:31]\n"
"s_add_u32 s57, s48, 28672\n"
"s_and_b32 s57, s57, 0x1ffff\n"
"buffer_load_dwordx4 v[208:211], v228, s[32:35], s57 offen offset:0 sc1\n"
"buffer_load_dwordx4 v[212:215], v228, s[32:35], s57 offen offset:1024 sc1\n"
"v_mfma_f32_32x32x16_bf16 a[48:63], v[148:151], a[220:223], a[48:63]\n"
"buffer_load_dwordx4 v[40:43], v228, s[32:35], s57 offen offset:2048 sc1\n"
"buffer_load_dwordx4 v[44:47], v228, s[32:35], s57 offen offset:3072 sc1\n"
"s_waitcnt vmcnt(17)\n"
"v_mfma_f32_32x32x16_bf16 a[0:15], v[152:155], v[48:51], a[0:15]\n"
"v_mfma_f32_32x32x16_bf16 a[32:47], v[156:159], v[48:51], a[32:47]\n"
"s_waitcnt vmcnt(16)\n"
"v_mfma_f32_32x32x16_bf16 a[16:31], v[152:155], v[52:55], a[16:31]\n"
"v_mfma_f32_32x32x16_bf16 a[48:63], v[156:159], v[52:55], a[48:63]\n"
"s_waitcnt lgkmcnt(2)\n"
"s_waitcnt vmcnt(15)\n"
"v_mfma_f32_32x32x16_bf16 a[0:15], v[16:19], a[200:203], a[0:15]\n"
"v_mfma_f32_32x32x16_bf16 a[32:47], v[20:23], a[200:203], a[32:47]\n"
"ds_read_b64_tr_b16 v[184:185], v226 offset:16384\n"
"ds_read_b64_tr_b16 v[186:187], v226 offset:17408\n"
"ds_read_b64_tr_b16 v[188:189], v250 offset:16384\n"
"ds_read_b64_tr_b16 v[190:191], v250 offset:17408\n"
"s_waitcnt vmcnt(14)\n"
"v_mfma_f32_32x32x16_bf16 a[16:31], v[16:19], a[204:207], a[16:31]\n"
"s_add_u32 s57, s48, 118784\n"
"s_and_b32 s57, s57, 0x1ffff\n"
"buffer_load_dwordx4 v[136:139], v228, s[32:35], s57 offen offset:0 sc1\n"
"buffer_load_dwordx4 v[140:143], v228, s[32:35], s57 offen offset:1024 sc1\n"
"v_mfma_f32_32x32x16_bf16 a[48:63], v[20:23], a[204:207], a[48:63]\n"
"buffer_load_dwordx4 v[0:3], v228, s[32:35], s57 offen offset:2048 sc1\n"
"buffer_load_dwordx4 v[4:7], v228, s[32:35], s57 offen offset:3072 sc1\n"
"s_waitcnt vmcnt(17)\n"
"v_mfma_f32_32x32x16_bf16 a[0:15], a[192:195], v[24:27], a[0:15]\n"
"s_waitcnt lgkmcnt(4)\n"
"v_mfma_f32_32x32x16_bf16 a[32:47], a[196:199], v[24:27], a[32:47]\n"
"ds_read_b64_tr_b16 a[224:225], v226 offset:18432\n"
"ds_read_b64_tr_b16 a[226:227], v226 offset:19456\n"
"ds_read_b64_tr_b16 a[228:229], v250 offset:18432\n"
"ds_read_b64_tr_b16 a[230:231], v250 offset:19456\n"
"s_waitcnt vmcnt(16)\n"
"v_mfma_f32_32x32x16_bf16 a[16:31], a[192:195], v[28:31], a[16:31]\n"
"v_mfma_f32_32x32x16_bf16 a[48:63], a[196:199], v[28:31], a[48:63]\n"
"s_waitcnt vmcnt(15)\n"
"v_mfma_f32_32x32x16_bf16 a[0:15], a[64:67], v[200:203], a[0:15]\n"
"v_mfma_f32_32x32x16_bf16 a[32:47], a[68:71], v[200:203], a[32:47]\n"
"s_waitcnt vmcnt(14)\n"
"v_mfma_f32_32x32x16_bf16 a[16:31], a[64:67], v[204:207], a[16:31]\n"
"s_add_u32 s57, s48, 32768\n"
"s_and_b32 s57, s57, 0x1ffff\n"
"buffer_load_dwordx4 a[208:211], v228, s[32:35], s57 offen offset:0 sc1\n"
"buffer_load_dwordx4 a[212:215], v228, s[32:35], s57 offen offset:1024 sc1\n"
"v_mfma_f32_32x32x16_bf16 a[48:63], a[68:71], v[204:207], a[48:63]\n"
"buffer_load_dwordx4 v[8:11], v228, s[32:35], s57 offen offset:2048 sc1\n"
"buffer_load_dwordx4 v[12:15], v228, s[32:35], s57 offen offset:3072 sc1\n"
"s_waitcnt vmcnt(17)\n"
"v_mfma_f32_32x32x16_bf16 a[0:15], a[72:75], v[32:35], a[0:15]\n"
"v_mfma_f32_32x32x16_bf16 a[32:47], a[76:79], v[32:35], a[32:47]\n"
"s_waitcnt vmcnt(16)\n"
"v_mfma_f32_32x32x16_bf16 a[16:31], a[72:75], v[36:39], a[16:31]\n"
"v_mfma_f32_32x32x16_bf16 a[48:63], a[76:79], v[36:39], a[48:63]\n"
"s_waitcnt lgkmcnt(2)\n"
"s_waitcnt vmcnt(15)\n"
"v_mfma_f32_32x32x16_bf16 a[0:15], v[184:187], a[232:235], a[0:15]\n"
"v_mfma_f32_32x32x16_bf16 a[32:47], v[188:191], a[232:235], a[32:47]\n"
"ds_read_b64_tr_b16 v[152:153], v224 offset:32768\n"
"ds_read_b64_tr_b16 v[154:155], v224 offset:33792\n"
"ds_read_b64_tr_b16 v[156:157], v225 offset:32768\n"
"ds_read_b64_tr_b16 v[158:159], v225 offset:33792\n"
"s_waitcnt vmcnt(14)\n"
"v_mfma_f32_32x32x16_bf16 a[16:31], v[184:187], a[236:239], a[16:31]\n"
"s_add_u32 s57, s48, 122880\n"
"s_and_b32 s57, s57, 0x1ffff\n"
"buffer_load_dwordx4 v[144:147], v228, s[32:35], s57 offen offset:0 sc1\n"
"buffer_load_dwordx4 v[148:151], v228, s[32:35], s57 offen offset:1024 sc1\n"
"v_mfma_f32_32x32x16_bf16 a[48:63], v[188:191], a[236:239], a[48:63]\n"
"buffer_load_dwordx4 a[216:219], v228, s[32:35], s57 offen offset:2048 sc1\n"
"buffer_load_dwordx4 a[220:223], v228, s[32:35], s57 offen offset:3072 sc1\n"
"s_waitcnt vmcnt(17)\n"
"v_mfma_f32_32x32x16_bf16 a[0:15], a[224:227], v[56:59], a[0:15]\n"
"s_waitcnt lgkmcnt(4)\n"
"v_mfma_f32_32x32x16_bf16 a[32:47], a[228:231], v[56:59], a[32:47]\n"
"ds_read_b64_tr_b16 v[48:49], v224 offset:34816\n"
"ds_read_b64_tr_b16 v[50:51], v224 offset:35840\n"
"ds_read_b64_tr_b16 v[52:53], v225 offset:34816\n"
"ds_read_b64_tr_b16 v[54:55], v225 offset:35840\n"
"s_waitcnt vmcnt(16)\n"
"v_mfma_f32_32x32x16_bf16 a[16:31], a[224:227], v[60:63], a[16:31]\n"
"v_mfma_f32_32x32x16_bf16 a[48:63], a[228:231], v[60:63], a[48:63]\n"
"s_waitcnt vmcnt(15)\n"
"v_mfma_f32_32x32x16_bf16 a[0:15], a[120:123], v[208:211], a[0:15]\n"
"v_mfma_f32_32x32x16_bf16 a[32:47], a[124:127], v[208:211], a[32:47]\n"
"s_waitcnt vmcnt(14)\n"
"v_mfma_f32_32x32x16_bf16 a[16:31], a[120:123], v[212:215], a[16:31]\n"
"s_add_u32 s57, s48, 36864\n"
"s_and_b32 s57, s57, 0x1ffff\n"
"buffer_load_dwordx4 v[16:19], v228, s[32:35], s57 offen offset:0 sc1\n"
"buffer_load_dwordx4 v[20:23], v228, s[32:35], s57 offen offset:1024 sc1\n"
"v_mfma_f32_32x32x16_bf16 a[48:63], a[124:127], v[212:215], a[48:63]\n"
"buffer_load_dwordx4 a[200:203], v228, s[32:35], s57 offen offset:2048 sc1\n"
"buffer_load_dwordx4 a[204:207], v228, s[32:35], s57 offen offset:3072 sc1\n"
"s_waitcnt vmcnt(17)\n"
"v_mfma_f32_32x32x16_bf16 a[0:15], a[144:147], v[40:43], a[0:15]\n"
"v_mfma_f32_32x32x16_bf16 a[32:47], a[148:151], v[40:43], a[32:47]\n"
"s_waitcnt vmcnt(16)\n"
"v_mfma_f32_32x32x16_bf16 a[16:31], a[144:147], v[44:47], a[16:31]\n"
"v_mfma_f32_32x32x16_bf16 a[48:63], a[148:151], v[44:47], a[48:63]\n"
"s_waitcnt lgkmcnt(2)\n"
"s_waitcnt vmcnt(15)\n"
"v_mfma_f32_32x32x16_bf16 a[0:15], v[152:155], v[136:139], a[0:15]\n"
"v_mfma_f32_32x32x16_bf16 a[32:47], v[156:159], v[136:139], a[32:47]\n"
"ds_read_b64_tr_b16 a[64:65], v251 offset:16384\n"
"ds_read_b64_tr_b16 a[66:67], v251 offset:17408\n"
"ds_read_b64_tr_b16 a[68:69], v255 offset:16384\n"
"ds_read_b64_tr_b16 a[70:71], v255 offset:17408\n"
"s_waitcnt vmcnt(14)\n"
"v_mfma_f32_32x32x16_bf16 a[16:31], v[152:155], v[140:143], a[16:31]\n"
"s_add_u32 s57, s48, 126976\n"
"s_and_b32 s57, s57, 0x1ffff\n"
"buffer_load_dwordx4 a[192:195], v228, s[32:35], s57 offen offset:0 sc1\n"
"buffer_load_dwordx4 a[196:199], v228, s[32:35], s57 offen offset:1024 sc1\n"
"v_mfma_f32_32x32x16_bf16 a[48:63], v[156:159], v[140:143], a[48:63]\n"
"buffer_load_dwordx4 v[24:27], v228, s[32:35], s57 offen offset:2048 sc1\n"
"buffer_load_dwordx4 v[28:31], v228, s[32:35], s57 offen offset:3072 sc1\n"
"s_waitcnt vmcnt(17)\n"
"v_mfma_f32_32x32x16_bf16 a[0:15], v[48:51], v[0:3], a[0:15]\n"
"s_waitcnt lgkmcnt(4)\n"
"v_mfma_f32_32x32x16_bf16 a[32:47], v[52:55], v[0:3], a[32:47]\n"
"ds_read_b64_tr_b16 v[200:201], v251 offset:18432\n"
"ds_read_b64_tr_b16 v[202:203], v251 offset:19456\n"
"ds_read_b64_tr_b16 v[204:205], v255 offset:18432\n"
"ds_read_b64_tr_b16 v[206:207], v255 offset:19456\n"
"s_waitcnt vmcnt(16)\n"
"v_mfma_f32_32x32x16_bf16 a[16:31], v[48:51], v[4:7], a[16:31]\n"
"v_mfma_f32_32x32x16_bf16 a[48:63], v[52:55], v[4:7], a[48:63]\n"
"s_waitcnt vmcnt(15)\n"
"v_mfma_f32_32x32x16_bf16 a[0:15], a[160:163], a[208:211], a[0:15]\n"
"v_mfma_f32_32x32x16_bf16 a[32:47], a[164:167], a[208:211], a[32:47]\n"
"s_waitcnt vmcnt(14)\n"
"v_mfma_f32_32x32x16_bf16 a[16:31], a[160:163], a[212:215], a[16:31]\n"
"s_add_u32 s57, s48, 40960\n"
"s_and_b32 s57, s57, 0x1ffff\n"
"buffer_load_dwordx4 a[72:75], v228, s[32:35], s57 offen offset:0 sc1\n"
"buffer_load_dwordx4 a[76:79], v228, s[32:35], s57 offen offset:1024 sc1\n"
"v_mfma_f32_32x32x16_bf16 a[48:63], a[164:167], a[212:215], a[48:63]\n"
"buffer_load_dwordx4 v[32:35], v228, s[32:35], s57 offen offset:2048 sc1\n"
"buffer_load_dwordx4 v[36:39], v228, s[32:35], s57 offen offset:3072 sc1\n"
"s_waitcnt vmcnt(17)\n"
"v_mfma_f32_32x32x16_bf16 a[0:15], a[168:171], v[8:11], a[0:15]\n"
"v_mfma_f32_32x32x16_bf16 a[32:47], a[172:175], v[8:11], a[32:47]\n"
"s_waitcnt vmcnt(16)\n"
"v_mfma_f32_32x32x16_bf16 a[16:31], a[168:171], v[12:15], a[16:31]\n"
"v_mfma_f32_32x32x16_bf16 a[48:63], a[172:175], v[12:15], a[48:63]\n"
"s_waitcnt lgkmcnt(2)\n"
"s_waitcnt vmcnt(15)\n"
"v_mfma_f32_32x32x16_bf16 a[0:15], a[64:67], v[144:147], a[0:15]\n"
"v_mfma_f32_32x32x16_bf16 a[32:47], a[68:71], v[144:147], a[32:47]\n"
"ds_read_b64_tr_b16 a[224:225], v224 offset:0\n"
"ds_read_b64_tr_b16 a[226:227], v224 offset:1024\n"
"ds_read_b64_tr_b16 a[228:229], v225 offset:0\n"
"ds_read_b64_tr_b16 a[230:231], v225 offset:1024\n"
"s_waitcnt vmcnt(14)\n"
"v_mfma_f32_32x32x16_bf16 a[16:31], a[64:67], v[148:151], a[16:31]\n"
"s_add_u32 s57, s48, 45056\n"
"s_and_b32 s57, s57, 0x1ffff\n"
"buffer_load_dwordx4 v[184:187], v228, s[32:35], s57 offen offset:0 sc1\n"
"buffer_load_dwordx4 v[188:191], v228, s[32:35], s57 offen offset:1024 sc1\n"
"v_mfma_f32_32x32x16_bf16 a[48:63], a[68:71], v[148:151], a[48:63]\n"
"buffer_load_dwordx4 a[232:235], v228, s[32:35], s57 offen offset:2048 sc1\n"
"buffer_load_dwordx4 a[236:239], v228, s[32:35], s57 offen offset:3072 sc1\n"
"s_waitcnt vmcnt(17)\n"
"v_mfma_f32_32x32x16_bf16 a[0:15], v[200:203], a[216:219], a[0:15]\n"
"s_waitcnt lgkmcnt(4)\n"
"v_mfma_f32_32x32x16_bf16 a[32:47], v[204:207], a[216:219], a[32:47]\n"
"ds_read_b64_tr_b16 v[56:57], v224 offset:2048\n"
"ds_read_b64_tr_b16 v[58:59], v224 offset:3072\n"
"ds_read_b64_tr_b16 v[60:61], v225 offset:2048\n"
"ds_read_b64_tr_b16 v[62:63], v225 offset:3072\n"
"s_waitcnt vmcnt(16)\n"
"v_mfma_f32_32x32x16_bf16 a[16:31], v[200:203], a[220:223], a[16:31]\n"
"v_mfma_f32_32x32x16_bf16 a[48:63], v[204:207], a[220:223], a[48:63]\n"
"s_waitcnt vmcnt(15)\n"
"v_mfma_f32_32x32x16_bf16 a[0:15], a[240:243], v[16:19], a[0:15]\n"
"v_mfma_f32_32x32x16_bf16 a[32:47], a[244:247], v[16:19], a[32:47]\n"
"s_waitcnt vmcnt(14)\n"
"v_mfma_f32_32x32x16_bf16 a[16:31], a[240:243], v[20:23], a[16:31]\n"
"s_add_u32 s57, s48, 49152\n"
"s_and_b32 s57, s57, 0x1ffff\n"
"buffer_load_dwordx4 a[120:123], v228, s[32:35], s57 offen offset:0 sc1\n"
"buffer_load_dwordx4 a[124:127], v228, s[32:35], s57 offen offset:1024 sc1\n"
"v_mfma_f32_32x32x16_bf16 a[48:63], a[244:247], v[20:23], a[48:63]\n"
"buffer_load_dwordx4 v[208:211], v228, s[32:35], s57 offen offset:2048 sc1\n"
"buffer_load_dwordx4 v[212:215], v228, s[32:35], s57 offen offset:3072 sc1\n"
"s_waitcnt vmcnt(17)\n"
"v_mfma_f32_32x32x16_bf16 a[0:15], a[248:251], a[200:203], a[0:15]\n"
"v_mfma_f32_32x32x16_bf16 a[32:47], a[252:255], a[200:203], a[32:47]\n"
"s_waitcnt vmcnt(16)\n"
"v_mfma_f32_32x32x16_bf16 a[16:31], a[248:251], a[204:207], a[16:31]\n"
"v_mfma_f32_32x32x16_bf16 a[48:63], a[252:255], a[204:207], a[48:63]\n"
"s_waitcnt lgkmcnt(2)\n"
"s_waitcnt vmcnt(15)\n"
"v_mfma_f32_32x32x16_bf16 a[0:15], a[224:227], a[192:195], a[0:15]\n"
"v_mfma_f32_32x32x16_bf16 a[32:47], a[228:231], a[192:195], a[32:47]\n"
"s_waitcnt vmcnt(14)\n"
"v_mfma_f32_32x32x16_bf16 a[16:31], a[224:227], a[196:199], a[16:31]\n"
"s_add_u32 s57, s48, 53248\n"
"s_and_b32 s57, s57, 0x1ffff\n"
"buffer_load_dwordx4 a[144:147], v228, s[32:35], s57 offen offset:0 sc1\n"
"buffer_load_dwordx4 a[148:151], v228, s[32:35], s57 offen offset:1024 sc1\n"
"v_mfma_f32_32x32x16_bf16 a[48:63], a[228:231], a[196:199], a[48:63]\n"
"buffer_load_dwordx4 v[40:43], v228, s[32:35], s57 offen offset:2048 sc1\n"
"buffer_load_dwordx4 v[44:47], v228, s[32:35], s57 offen offset:3072 sc1\n"
"s_waitcnt vmcnt(17)\n"
"v_mfma_f32_32x32x16_bf16 a[0:15], v[56:59], v[24:27], a[0:15]\n"
"s_waitcnt lgkmcnt(0)\n"
"v_mfma_f32_32x32x16_bf16 a[32:47], v[60:63], v[24:27], a[32:47]\n"
"s_waitcnt vmcnt(16)\n"
"v_mfma_f32_32x32x16_bf16 a[16:31], v[56:59], v[28:31], a[16:31]\n"
"v_mfma_f32_32x32x16_bf16 a[48:63], v[60:63], v[28:31], a[48:63]\n"
"s_waitcnt vmcnt(15)\n"
"v_mfma_f32_32x32x16_bf16 a[0:15], v[160:163], a[72:75], a[0:15]\n"
"v_mfma_f32_32x32x16_bf16 a[32:47], v[164:167], a[72:75], a[32:47]\n"
"s_waitcnt vmcnt(14)\n"
"v_mfma_f32_32x32x16_bf16 a[16:31], v[160:163], a[76:79], a[16:31]\n"
"s_add_u32 s57, s48, 57344\n"
"s_and_b32 s57, s57, 0x1ffff\n"
"buffer_load_dwordx4 v[152:155], v228, s[32:35], s57 offen offset:0 sc1\n"
"buffer_load_dwordx4 v[156:159], v228, s[32:35], s57 offen offset:1024 sc1\n"
"v_mfma_f32_32x32x16_bf16 a[48:63], v[164:167], a[76:79], a[48:63]\n"
"buffer_load_dwordx4 v[136:139], v228, s[32:35], s57 offen offset:2048 sc1\n"
"buffer_load_dwordx4 v[140:143], v228, s[32:35], s57 offen offset:3072 sc1\n"
"s_waitcnt vmcnt(17)\n"
"v_mfma_f32_32x32x16_bf16 a[0:15], v[168:171], v[32:35], a[0:15]\n"
"v_mfma_f32_32x32x16_bf16 a[32:47], v[172:175], v[32:35], a[32:47]\n"
"s_waitcnt vmcnt(16)\n"
"v_mfma_f32_32x32x16_bf16 a[16:31], v[168:171], v[36:39], a[16:31]\n"
"v_mfma_f32_32x32x16_bf16 a[48:63], v[172:175], v[36:39], a[48:63]\n"
"s_waitcnt vmcnt(15)\n"
"v_mfma_f32_32x32x16_bf16 a[0:15], v[176:179], v[184:187], a[0:15]\n"
"v_mfma_f32_32x32x16_bf16 a[32:47], v[180:183], v[184:187], a[32:47]\n"
"s_waitcnt vmcnt(14)\n"
"v_mfma_f32_32x32x16_bf16 a[16:31], v[176:179], v[188:191], a[16:31]\n"
"s_add_u32 s57, s48, 61440\n"
"s_and_b32 s57, s57, 0x1ffff\n"
"buffer_load_dwordx4 v[48:51], v228, s[32:35], s57 offen offset:0 sc1\n"
"buffer_load_dwordx4 v[52:55], v228, s[32:35], s57 offen offset:1024 sc1\n"
"v_mfma_f32_32x32x16_bf16 a[48:63], v[180:183], v[188:191], a[48:63]\n"
"buffer_load_dwordx4 v[0:3], v228, s[32:35], s57 offen offset:2048 sc1\n"
"buffer_load_dwordx4 v[4:7], v228, s[32:35], s57 offen offset:3072 sc1\n"
"s_waitcnt vmcnt(17)\n"
"v_mfma_f32_32x32x16_bf16 a[0:15], v[192:195], a[232:235], a[0:15]\n"
"v_mfma_f32_32x32x16_bf16 a[32:47], v[196:199], a[232:235], a[32:47]\n"
"s_waitcnt vmcnt(16)\n"
"v_mfma_f32_32x32x16_bf16 a[16:31], v[192:195], a[236:239], a[16:31]\n"
"v_mfma_f32_32x32x16_bf16 a[48:63], v[196:199], a[236:239], a[48:63]\n"
"s_waitcnt vmcnt(15)\n"
"v_mfma_f32_32x32x16_bf16 a[0:15], v[216:219], a[120:123], a[0:15]\n"
"v_mfma_f32_32x32x16_bf16 a[32:47], v[220:223], a[120:123], a[32:47]\n"
"s_waitcnt vmcnt(14)\n"
"v_mfma_f32_32x32x16_bf16 a[16:31], v[216:219], a[124:127], a[16:31]\n"
"s_add_u32 s57, s48, 65536\n"
"s_and_b32 s57, s57, 0x1ffff\n"
"buffer_load_dwordx4 a[160:163], v228, s[32:35], s57 offen offset:0 sc1\n"
"buffer_load_dwordx4 a[164:167], v228, s[32:35], s57 offen offset:1024 sc1\n"
"v_mfma_f32_32x32x16_bf16 a[48:63], v[220:223], a[124:127], a[48:63]\n"
"buffer_load_dwordx4 a[208:211], v228, s[32:35], s57 offen offset:2048 sc1\n"
"buffer_load_dwordx4 a[212:215], v228, s[32:35], s57 offen offset:3072 sc1\n"
"s_waitcnt vmcnt(17)\n"
"v_mfma_f32_32x32x16_bf16 a[0:15], v[128:131], v[208:211], a[0:15]\n"
"v_mfma_f32_32x32x16_bf16 a[32:47], v[132:135], v[208:211], a[32:47]\n"
"s_waitcnt vmcnt(16)\n"
"v_mfma_f32_32x32x16_bf16 a[16:31], v[128:131], v[212:215], a[16:31]\n"
"v_mfma_f32_32x32x16_bf16 a[48:63], v[132:135], v[212:215], a[48:63]\n"
"s_waitcnt vmcnt(15)\n"
"v_mfma_f32_32x32x16_bf16 a[0:15], a[80:83], a[144:147], a[0:15]\n"
"v_mfma_f32_32x32x16_bf16 a[32:47], a[84:87], a[144:147], a[32:47]\n"
"s_waitcnt vmcnt(14)\n"
"v_mfma_f32_32x32x16_bf16 a[16:31], a[80:83], a[148:151], a[16:31]\n"
"s_add_u32 s57, s48, 69632\n"
"s_and_b32 s57, s57, 0x1ffff\n"
"buffer_load_dwordx4 a[168:171], v228, s[32:35], s57 offen offset:0 sc1\n"
"buffer_load_dwordx4 a[172:175], v228, s[32:35], s57 offen offset:1024 sc1\n"
"v_mfma_f32_32x32x16_bf16 a[48:63], a[84:87], a[148:151], a[48:63]\n"
"buffer_load_dwordx4 v[8:11], v228, s[32:35], s57 offen offset:2048 sc1\n"
"buffer_load_dwordx4 v[12:15], v228, s[32:35], s57 offen offset:3072 sc1\n"
"s_waitcnt vmcnt(17)\n"
"v_mfma_f32_32x32x16_bf16 a[0:15], a[88:91], v[40:43], a[0:15]\n"
"v_mfma_f32_32x32x16_bf16 a[32:47], a[92:95], v[40:43], a[32:47]\n"
"s_waitcnt vmcnt(16)\n"
"v_mfma_f32_32x32x16_bf16 a[16:31], a[88:91], v[44:47], a[16:31]\n"
"v_mfma_f32_32x32x16_bf16 a[48:63], a[92:95], v[44:47], a[48:63]\n"
"s_waitcnt vmcnt(15)\n"
"v_mfma_f32_32x32x16_bf16 a[0:15], a[96:99], v[152:155], a[0:15]\n"
"v_mfma_f32_32x32x16_bf16 a[32:47], a[100:103], v[152:155], a[32:47]\n"
"s_waitcnt vmcnt(14)\n"
"v_mfma_f32_32x32x16_bf16 a[16:31], a[96:99], v[156:159], a[16:31]\n"
"s_add_u32 s57, s48, 73728\n"
"s_and_b32 s57, s57, 0x1ffff\n"
"buffer_load_dwordx4 a[64:67], v228, s[32:35], s57 offen offset:0 sc1\n"
"buffer_load_dwordx4 v[144:147], v228, s[32:35], s57 offen offset:2048 sc1\n"
"v_mfma_f32_32x32x16_bf16 a[48:63], a[100:103], v[156:159], a[48:63]\n"
"s_add_u32 s57, s48, 77824\n"
"s_and_b32 s57, s57, 0x1ffff\n"
"buffer_load_dwordx4 v[200:203], v228, s[32:35], s57 offen offset:0 sc1\n"
"buffer_load_dwordx4 a[216:219], v228, s[32:35], s57 offen offset:2048 sc1\n"
"s_waitcnt vmcnt(17)\n"
"v_mfma_f32_32x32x16_bf16 a[0:15], a[104:107], v[136:139], a[0:15]\n"
"v_mfma_f32_32x32x16_bf16 a[32:47], a[108:111], v[136:139], a[32:47]\n"
"s_waitcnt vmcnt(16)\n"
"v_mfma_f32_32x32x16_bf16 a[16:31], a[104:107], v[140:143], a[16:31]\n"
"v_mfma_f32_32x32x16_bf16 a[48:63], a[108:111], v[140:143], a[48:63]\n"
"s_waitcnt vmcnt(15)\n"
"v_mfma_f32_32x32x16_bf16 a[0:15], a[112:115], v[48:51], a[0:15]\n"
"v_mfma_f32_32x32x16_bf16 a[32:47], a[116:119], v[48:51], a[32:47]\n"
"s_waitcnt vmcnt(14)\n"
"v_mfma_f32_32x32x16_bf16 a[16:31], a[112:115], v[52:55], a[16:31]\n"
"s_add_u32 s57, s48, 81920\n"
"s_and_b32 s57, s57, 0x1ffff\n"
"buffer_load_dwordx4 a[240:243], v228, s[32:35], s57 offen offset:0 sc1\n"
"buffer_load_dwordx4 v[16:19], v228, s[32:35], s57 offen offset:2048 sc1\n"
"v_mfma_f32_32x32x16_bf16 a[48:63], a[116:119], v[52:55], a[48:63]\n"
"s_add_u32 s57, s48, 86016\n"
"s_and_b32 s57, s57, 0x1ffff\n"
"buffer_load_dwordx4 a[248:251], v228, s[32:35], s57 offen offset:0 sc1\n"
"buffer_load_dwordx4 a[200:203], v228, s[32:35], s57 offen offset:2048 sc1\n"
"s_waitcnt vmcnt(17)\n"
"v_mfma_f32_32x32x16_bf16 a[0:15], a[128:131], v[0:3], a[0:15]\n"
"v_mfma_f32_32x32x16_bf16 a[32:47], a[132:135], v[0:3], a[32:47]\n"
"s_waitcnt vmcnt(16)\n"
"v_mfma_f32_32x32x16_bf16 a[16:31], a[128:131], v[4:7], a[16:31]\n"
"v_mfma_f32_32x32x16_bf16 a[48:63], a[132:135], v[4:7], a[48:63]\n"
"s_waitcnt vmcnt(15)\n"
"v_mfma_f32_32x32x16_bf16 a[0:15], a[136:139], a[160:163], a[0:15]\n"
"v_mfma_f32_32x32x16_bf16 a[32:47], a[140:143], a[160:163], a[32:47]\n"
"s_waitcnt vmcnt(14)\n"
"v_mfma_f32_32x32x16_bf16 a[16:31], a[136:139], a[164:167], a[16:31]\n"
"s_add_u32 s57, s48, 73728\n"
"s_and_b32 s57, s57, 0x1ffff\n"
"buffer_load_dwordx4 a[68:71], v228, s[32:35], s57 offen offset:1024 sc1\n"
"buffer_load_dwordx4 v[148:151], v228, s[32:35], s57 offen offset:3072 sc1\n"
"v_mfma_f32_32x32x16_bf16 a[48:63], a[140:143], a[164:167], a[48:63]\n"
"s_add_u32 s57, s48, 77824\n"
"s_and_b32 s57, s57, 0x1ffff\n"
"buffer_load_dwordx4 v[204:207], v228, s[32:35], s57 offen offset:1024 sc1\n"
"buffer_load_dwordx4 a[220:223], v228, s[32:35], s57 offen offset:3072 sc1\n"
"s_waitcnt vmcnt(17)\n"
"v_mfma_f32_32x32x16_bf16 a[0:15], a[152:155], a[208:211], a[0:15]\n"
"v_mfma_f32_32x32x16_bf16 a[32:47], a[156:159], a[208:211], a[32:47]\n"
"s_waitcnt vmcnt(16)\n"
"v_mfma_f32_32x32x16_bf16 a[16:31], a[152:155], a[212:215], a[16:31]\n"
"v_mfma_f32_32x32x16_bf16 a[48:63], a[156:159], a[212:215], a[48:63]\n"
"s_waitcnt vmcnt(15)\n"
"v_mfma_f32_32x32x16_bf16 a[0:15], a[176:179], a[168:171], a[0:15]\n"
"v_mfma_f32_32x32x16_bf16 a[32:47], a[180:183], a[168:171], a[32:47]\n"
"s_waitcnt vmcnt(14)\n"
"v_mfma_f32_32x32x16_bf16 a[16:31], a[176:179], a[172:175], a[16:31]\n"
"s_add_u32 s57, s48, 81920\n"
"s_and_b32 s57, s57, 0x1ffff\n"
"buffer_load_dwordx4 a[244:247], v228, s[32:35], s57 offen offset:1024 sc1\n"
"buffer_load_dwordx4 v[20:23], v228, s[32:35], s57 offen offset:3072 sc1\n"
"v_mfma_f32_32x32x16_bf16 a[48:63], a[180:183], a[172:175], a[48:63]\n"
"s_add_u32 s57, s48, 86016\n"
"s_and_b32 s57, s57, 0x1ffff\n"
"buffer_load_dwordx4 a[252:255], v228, s[32:35], s57 offen offset:1024 sc1\n"
"buffer_load_dwordx4 a[204:207], v228, s[32:35], s57 offen offset:3072 sc1\n"
"s_waitcnt vmcnt(17)\n"
"v_mfma_f32_32x32x16_bf16 a[0:15], a[184:187], v[8:11], a[0:15]\n"
"v_mfma_f32_32x32x16_bf16 a[32:47], a[188:191], v[8:11], a[32:47]\n"
"s_waitcnt vmcnt(16)\n"
"v_mfma_f32_32x32x16_bf16 a[16:31], a[184:187], v[12:15], a[16:31]\n"
"v_mfma_f32_32x32x16_bf16 a[48:63], a[188:191], v[12:15], a[48:63]\n"
"v_lshrrev_b32 v251, 4, v228\n"
"v_bfe_u32 v235, v251, 4, 1\n"
"v_and_b32 v236, 15, v251\n"
"v_lshrrev_b32 v237, 5, v251\n"
"v_and_b32 v238, 7, v251\n"
"v_xor_b32 v237, v237, v238\n"
"v_lshlrev_b32 v237, 4, v237\n"
"v_lshl_add_u32 v237, v236, 8, v237\n"
"v_lshl_add_u32 v237, v235, 14, v237\n"
"v_add_u32 v229, s45, v237\n"
"v_xor_b32 v230, 32, v229\n"
"v_xor_b32 v231, 64, v229\n"
"v_xor_b32 v232, 96, v229\n"
"v_lshrrev_b32 v235, 2, v251\n"
"v_and_b32 v236, 3, v251\n"
"v_lshlrev_b32 v237, 2, v236\n"
"v_and_b32 v238, 7, v235\n"
"v_xor_b32 v237, v237, v238\n"
"v_lshlrev_b32 v237, 4, v237\n"
"v_lshl_add_u32 v237, v235, 8, v237\n"
"s_lshl_b32 s56, s44, 14\n"
"v_add_u32 v239, s56, v237\n"
"v_xor_b32 v240, 16, v239\n"
"v_xor_b32 v241, 32, v239\n"
"v_xor_b32 v242, 48, v239\n"
"s_and_b32 s57, s55, 3\n"
"s_lshl_b32 s57, s57, 16\n"
"s_add_u32 s56, s56, s57\n"
"s_add_u32 s56, s56, s84\n"
"v_lshlrev_b32 v243, 10, v235\n"
"v_lshl_add_u32 v243, v236, 5, v243\n"
"v_add_u32 v243, s56, v243\n"
"s_waitcnt vmcnt(15)\n"
"v_mfma_f32_32x32x16_bf16 a[0:15], v[64:67], a[64:67], a[0:15]\n"
"v_mfma_f32_32x32x16_bf16 a[32:47], v[68:71], a[64:67], a[32:47]\n"
"s_waitcnt vmcnt(14)\n"
"v_mfma_f32_32x32x16_bf16 a[0:15], v[72:75], v[144:147], a[0:15]\n"
"v_mfma_f32_32x32x16_bf16 a[32:47], v[76:79], v[144:147], a[32:47]\n"
"s_waitcnt vmcnt(13)\n"
"v_mfma_f32_32x32x16_bf16 a[0:15], v[80:83], v[200:203], a[0:15]\n"
"v_mfma_f32_32x32x16_bf16 a[32:47], v[84:87], v[200:203], a[32:47]\n"
"s_waitcnt vmcnt(12)\n"
"v_mfma_f32_32x32x16_bf16 a[0:15], v[88:91], a[216:219], a[0:15]\n"
"v_mfma_f32_32x32x16_bf16 a[32:47], v[92:95], a[216:219], a[32:47]\n"
"s_waitcnt vmcnt(11)\n"
"v_mfma_f32_32x32x16_bf16 a[0:15], v[96:99], a[240:243], a[0:15]\n"
"v_mfma_f32_32x32x16_bf16 a[32:47], v[100:103], a[240:243], a[32:47]\n"
"s_waitcnt vmcnt(10)\n"
"v_mfma_f32_32x32x16_bf16 a[0:15], v[104:107], v[16:19], a[0:15]\n"
"v_mfma_f32_32x32x16_bf16 a[32:47], v[108:111], v[16:19], a[32:47]\n"
"s_waitcnt vmcnt(9)\n"
"v_mfma_f32_32x32x16_bf16 a[0:15], v[112:115], a[248:251], a[0:15]\n"
"v_mfma_f32_32x32x16_bf16 a[32:47], v[116:119], a[248:251], a[32:47]\n"
"s_waitcnt vmcnt(8)\n"
"v_mfma_f32_32x32x16_bf16 a[0:15], v[120:123], a[200:203], a[0:15]\n"
"v_mfma_f32_32x32x16_bf16 a[32:47], v[124:127], a[200:203], a[32:47]\n"
"s_nop 10\n"
"ds_write_b128 v229, a[0:3] offset:0\n"
"ds_write_b128 v230, a[4:7] offset:0\n"
"ds_write_b128 v231, a[8:11] offset:0\n"
"ds_write_b128 v232, a[12:15] offset:0\n"
"ds_write_b128 v229, a[32:35] offset:128\n"
"ds_write_b128 v230, a[36:39] offset:128\n"
"ds_write_b128 v231, a[40:43] offset:128\n"
"ds_write_b128 v232, a[44:47] offset:128\n"
"s_waitcnt vmcnt(7)\n"
"v_mfma_f32_32x32x16_bf16 a[16:31], v[64:67], a[68:71], a[16:31]\n"
"v_mfma_f32_32x32x16_bf16 a[48:63], v[68:71], a[68:71], a[48:63]\n"
"s_waitcnt vmcnt(6)\n"
"v_mfma_f32_32x32x16_bf16 a[16:31], v[72:75], v[148:151], a[16:31]\n"
"v_mfma_f32_32x32x16_bf16 a[48:63], v[76:79], v[148:151], a[48:63]\n"
"s_waitcnt vmcnt(5)\n"
"v_mfma_f32_32x32x16_bf16 a[16:31], v[80:83], v[204:207], a[16:31]\n"
"v_mfma_f32_32x32x16_bf16 a[48:63], v[84:87], v[204:207], a[48:63]\n"
"s_waitcnt vmcnt(4)\n"
"v_mfma_f32_32x32x16_bf16 a[16:31], v[88:91], a[220:223], a[16:31]\n"
"v_mfma_f32_32x32x16_bf16 a[48:63], v[92:95], a[220:223], a[48:63]\n"
"s_waitcnt vmcnt(3)\n"
"v_mfma_f32_32x32x16_bf16 a[16:31], v[96:99], a[244:247], a[16:31]\n"
"v_mfma_f32_32x32x16_bf16 a[48:63], v[100:103], a[244:247], a[48:63]\n"
"s_waitcnt vmcnt(2)\n"
"v_mfma_f32_32x32x16_bf16 a[16:31], v[104:107], v[20:23], a[16:31]\n"
"v_mfma_f32_32x32x16_bf16 a[48:63], v[108:111], v[20:23], a[48:63]\n"
"s_waitcnt vmcnt(1)\n"
"v_mfma_f32_32x32x16_bf16 a[16:31], v[112:115], a[252:255], a[16:31]\n"
"v_mfma_f32_32x32x16_bf16 a[48:63], v[116:119], a[252:255], a[48:63]\n"
"s_waitcnt vmcnt(0)\n"
"v_mfma_f32_32x32x16_bf16 a[16:31], v[120:123], a[204:207], a[16:31]\n"
"v_mfma_f32_32x32x16_bf16 a[48:63], v[124:127], a[204:207], a[48:63]\n"
"s_nop 10\n"
"ds_write_b128 v229, a[16:19] offset:32768\n"
"ds_write_b128 v230, a[20:23] offset:32768\n"
"ds_write_b128 v231, a[24:27] offset:32768\n"
"ds_write_b128 v232, a[28:31] offset:32768\n"
"ds_write_b128 v229, a[48:51] offset:32896\n"
"ds_write_b128 v230, a[52:55] offset:32896\n"
"ds_write_b128 v231, a[56:59] offset:32896\n"
"ds_write_b128 v232, a[60:63] offset:32896\n"
"s_waitcnt lgkmcnt(0)\n"
"s_barrier\n"
"ds_read_b128 v[16:19], v239 offset:0\n"
"ds_read_b128 v[20:23], v239 offset:4096\n"
"ds_read_b128 v[24:27], v239 offset:8192\n"
"ds_read_b128 v[28:31], v239 offset:12288\n"
"ds_read_b128 v[32:35], v240 offset:0\n"
"ds_read_b128 v[36:39], v240 offset:4096\n"
"ds_read_b128 v[40:43], v240 offset:8192\n"
"ds_read_b128 v[44:47], v240 offset:12288\n"
"ds_read_b128 v[48:51], v241 offset:0\n"
"ds_read_b128 v[52:55], v241 offset:4096\n"
"ds_read_b128 v[56:59], v241 offset:8192\n"
"ds_read_b128 v[60:63], v241 offset:12288\n"
"ds_read_b128 v[64:67], v242 offset:0\n"
"ds_read_b128 v[68:71], v242 offset:4096\n"
"ds_read_b128 v[72:75], v242 offset:8192\n"
"ds_read_b128 v[76:79], v242 offset:12288\n"
"s_waitcnt lgkmcnt(14)\n"
"v_add_f32 v16, v16, v20\n"
"s_waitcnt lgkmcnt(12)\n"
"v_add_f32 v24, v24, v28\n"
"v_add_f32 v17, v17, v21\n"
"v_add_f32 v25, v25, v29\n"
"v_add_f32 v18, v18, v22\n"
"v_add_f32 v26, v26, v30\n"
"v_add_f32 v19, v19, v23\n"
"v_add_f32 v27, v27, v31\n"
"v_add_f32 v16, v16, v24\n"
"v_add_f32 v17, v17, v25\n"
"v_add_f32 v18, v18, v26\n"
"v_add_f32 v19, v19, v27\n"
"v_cvt_pk_bf16_f32 v80, v16, v17\n"
"v_cvt_pk_bf16_f32 v81, v18, v19\n"
"s_waitcnt lgkmcnt(10)\n"
"v_add_f32 v32, v32, v36\n"
"s_waitcnt lgkmcnt(8)\n"
"v_add_f32 v40, v40, v44\n"
"v_add_f32 v33, v33, v37\n"
"v_add_f32 v41, v41, v45\n"
"v_add_f32 v34, v34, v38\n"
"v_add_f32 v42, v42, v46\n"
"v_add_f32 v35, v35, v39\n"
"v_add_f32 v43, v43, v47\n"
"v_add_f32 v32, v32, v40\n"
"v_add_f32 v33, v33, v41\n"
"v_add_f32 v34, v34, v42\n"
"v_add_f32 v35, v35, v43\n"
"v_cvt_pk_bf16_f32 v82, v32, v33\n"
"v_cvt_pk_bf16_f32 v83, v34, v35\n"
"buffer_store_dwordx4 v[80:83], v243, s[36:39], 0 offen offset:0\n"
"s_waitcnt lgkmcnt(6)\n"
"v_add_f32 v48, v48, v52\n"
"s_waitcnt lgkmcnt(4)\n"
"v_add_f32 v56, v56, v60\n"
"v_add_f32 v49, v49, v53\n"
"v_add_f32 v57, v57, v61\n"
"v_add_f32 v50, v50, v54\n"
"v_add_f32 v58, v58, v62\n"
"v_add_f32 v51, v51, v55\n"
"v_add_f32 v59, v59, v63\n"
"v_add_f32 v48, v48, v56\n"
"v_add_f32 v49, v49, v57\n"
"v_add_f32 v50, v50, v58\n"
"v_add_f32 v51, v51, v59\n"
"v_cvt_pk_bf16_f32 v84, v48, v49\n"
"v_cvt_pk_bf16_f32 v85, v50, v51\n"
"s_waitcnt lgkmcnt(2)\n"
"v_add_f32 v64, v64, v68\n"
"s_waitcnt lgkmcnt(0)\n"
"v_add_f32 v72, v72, v76\n"
"v_add_f32 v65, v65, v69\n"
"v_add_f32 v73, v73, v77\n"
"v_add_f32 v66, v66, v70\n"
"v_add_f32 v74, v74, v78\n"
"v_add_f32 v67, v67, v71\n"
"v_add_f32 v75, v75, v79\n"
"v_add_f32 v64, v64, v72\n"
"v_add_f32 v65, v65, v73\n"
"v_add_f32 v66, v66, v74\n"
"v_add_f32 v67, v67, v75\n"
"v_cvt_pk_bf16_f32 v86, v64, v65\n"
"v_cvt_pk_bf16_f32 v87, v66, v67\n"
"buffer_store_dwordx4 v[84:87], v243, s[36:39], 0 offen offset:16\n"
"s_endpgm\n"
"Lstub1_%=:\n"
"s_getpc_b64 s[50:51]\n"
"s_branch Lslow1_%=\n"
"s_branch Lret1_%=\n"
"Lstub22_%=:\n"
"s_getpc_b64 s[50:51]\n"
"s_branch Lresc_%=\n"
"s_branch Lret22_%=\n"
"Lstub2_%=:\n"
"s_getpc_b64 s[50:51]\n"
"s_branch Lslow0_%=\n"
"s_branch Lret2_%=\n"
"Lstub23_%=:\n"
"s_getpc_b64 s[50:51]\n"
"s_branch Lresc_%=\n"
"s_branch Lret23_%=\n"
"Lstub3_%=:\n"
"s_getpc_b64 s[50:51]\n"
"s_branch Lslow1_%=\n"
"s_branch Lret3_%=\n"
"Lstub24_%=:\n"
"s_getpc_b64 s[50:51]\n"
"s_branch Lresc_%=\n"
"s_branch Lret24_%=\n"
"Lstub4_%=:\n"
"s_getpc_b64 s[50:51]\n"
"s_branch Lslow0_%=\n"
"s_branch Lret4_%=\n"
"Lstub25_%=:\n"
"s_getpc_b64 s[50:51]\n"
"s_branch Lresc_%=\n"
"s_branch Lret25_%=\n"
"Lstub5_%=:\n"
"s_getpc_b64 s[50:51]\n"
"s_branch Lslow1_%=\n"
"s_branch Lret5_%=\n"
"Lstub26_%=:\n"
"s_getpc_b64 s[50:51]\n"
"s_branch Lresc_%=\n"
"s_branch Lret26_%=\n"
"Lstub6_%=:\n"
"s_getpc_b64 s[50:51]\n"
"s_branch Lslow0_%=\n"
"s_branch Lret6_%=\n"
"Lstub27_%=:\n"
"s_getpc_b64 s[50:51]\n"
"s_branch Lresc_%=\n"
"s_branch Lret27_%=\n"
"Lstub7_%=:\n"
"s_getpc_b64 s[50:51]\n"
"s_branch Lslow1_%=\n"
"s_branch Lret7_%=\n"
"Lstub28_%=:\n"
"s_getpc_b64 s[50:51]\n"
"s_branch Lresc_%=\n"
"s_branch Lret28_%=\n"
"Lslow0_%=:\n"
"s_nop 15\n"
"v_max_f32 v241, v0, v1\n"
"v_max3_f32 v241, v241, v2, v3\n"
"v_max3_f32 v241, v241, v4, v5\n"
"v_max3_f32 v241, v241, v6, v7\n"
"v_max3_f32 v241, v241, v8, v9\n"
"v_max3_f32 v241, v241, v10, v11\n"
"v_max3_f32 v241, v241, v12, v13\n"
"v_max3_f32 v241, v241, v14, v15\n"
"v_max3_f32 v241, v241, v32, v33\n"
"v_max3_f32 v241, v241, v34, v35\n"
"v_max3_f32 v241, v241, v36, v37\n"
"v_max3_f32 v241, v241, v38, v39\n"
"v_max3_f32 v241, v241, v40, v41\n"
"v_max3_f32 v241, v241, v42, v43\n"
"v_max3_f32 v241, v241, v44, v45\n"
"v_max3_f32 v241, v241, v46, v47\n"
"v_mov_b32 v242, v241\n"
"s_nop 1\n"
"v_permlane32_swap_b32 v241, v242\n"
"v_max3_f32 v241, v241, v242, 0\n"
"v_sub_f32 v242, 0, v241\n"
"v_exp_f32 v243, v242\n"
"v_sub_f32 v0, v0, v241\n"
"v_sub_f32 v1, v1, v241\n"
"v_sub_f32 v2, v2, v241\n"
"v_sub_f32 v3, v3, v241\n"
"v_sub_f32 v4, v4, v241\n"
"v_sub_f32 v5, v5, v241\n"
"v_sub_f32 v6, v6, v241\n"
"v_sub_f32 v7, v7, v241\n"
"v_sub_f32 v8, v8, v241\n"
"v_sub_f32 v9, v9, v241\n"
"v_sub_f32 v10, v10, v241\n"
"v_sub_f32 v11, v11, v241\n"
"v_sub_f32 v12, v12, v241\n"
"v_sub_f32 v13, v13, v241\n"
"v_sub_f32 v14, v14, v241\n"
"v_sub_f32 v15, v15, v241\n"
"v_sub_f32 v32, v32, v241\n"
"v_sub_f32 v33, v33, v241\n"
"v_sub_f32 v34, v34, v241\n"
"v_sub_f32 v35, v35, v241\n"
"v_sub_f32 v36, v36, v241\n"
"v_sub_f32 v37, v37, v241\n"
"v_sub_f32 v38, v38, v241\n"
"v_sub_f32 v39, v39, v241\n"
"v_sub_f32 v40, v40, v241\n"
"v_sub_f32 v41, v41, v241\n"
"v_sub_f32 v42, v42, v241\n"
"v_sub_f32 v43, v43, v241\n"
"v_sub_f32 v44, v44, v241\n"
"v_sub_f32 v45, v45, v241\n"
"v_sub_f32 v46, v46, v241\n"
"v_sub_f32 v47, v47, v241\n"
"v_sub_f32 v128, v128, v241\n"
"v_sub_f32 v129, v129, v241\n"
"v_sub_f32 v130, v130, v241\n"
"v_sub_f32 v131, v131, v241\n"
"v_sub_f32 v132, v132, v241\n"
"v_sub_f32 v133, v133, v241\n"
"v_sub_f32 v134, v134, v241\n"
"v_sub_f32 v135, v135, v241\n"
"v_sub_f32 v136, v136, v241\n"
"v_sub_f32 v137, v137, v241\n"
"v_sub_f32 v138, v138, v241\n"
"v_sub_f32 v139, v139, v241\n"
"v_sub_f32 v140, v140, v241\n"
"v_sub_f32 v141, v141, v241\n"
"v_sub_f32 v142, v142, v241\n"
"v_sub_f32 v143, v143, v241\n"
"v_mul_f32 v235, v243, v235\n"
"v_mul_f32 v236, v243, v236\n"
"v_max_f32 v241, v16, v17\n"
"v_max3_f32 v241, v241, v18, v19\n"
"v_max3_f32 v241, v241, v20, v21\n"
"v_max3_f32 v241, v241, v22, v23\n"
"v_max3_f32 v241, v241, v24, v25\n"
"v_max3_f32 v241, v241, v26, v27\n"
"v_max3_f32 v241, v241, v28, v29\n"
"v_max3_f32 v241, v241, v30, v31\n"
"v_max3_f32 v241, v241, v48, v49\n"
"v_max3_f32 v241, v241, v50, v51\n"
"v_max3_f32 v241, v241, v52, v53\n"
"v_max3_f32 v241, v241, v54, v55\n"
"v_max3_f32 v241, v241, v56, v57\n"
"v_max3_f32 v241, v241, v58, v59\n"
"v_max3_f32 v241, v241, v60, v61\n"
"v_max3_f32 v241, v241, v62, v63\n"
"v_mov_b32 v242, v241\n"
"s_nop 1\n"
"v_permlane32_swap_b32 v241, v242\n"
"v_max3_f32 v241, v241, v242, 0\n"
"v_sub_f32 v242, 0, v241\n"
"v_exp_f32 v244, v242\n"
"v_sub_f32 v16, v16, v241\n"
"v_sub_f32 v17, v17, v241\n"
"v_sub_f32 v18, v18, v241\n"
"v_sub_f32 v19, v19, v241\n"
"v_sub_f32 v20, v20, v241\n"
"v_sub_f32 v21, v21, v241\n"
"v_sub_f32 v22, v22, v241\n"
"v_sub_f32 v23, v23, v241\n"
"v_sub_f32 v24, v24, v241\n"
"v_sub_f32 v25, v25, v241\n"
"v_sub_f32 v26, v26, v241\n"
"v_sub_f32 v27, v27, v241\n"
"v_sub_f32 v28, v28, v241\n"
"v_sub_f32 v29, v29, v241\n"
"v_sub_f32 v30, v30, v241\n"
"v_sub_f32 v31, v31, v241\n"
"v_sub_f32 v48, v48, v241\n"
"v_sub_f32 v49, v49, v241\n"
"v_sub_f32 v50, v50, v241\n"
"v_sub_f32 v51, v51, v241\n"
"v_sub_f32 v52, v52, v241\n"
"v_sub_f32 v53, v53, v241\n"
"v_sub_f32 v54, v54, v241\n"
"v_sub_f32 v55, v55, v241\n"
"v_sub_f32 v56, v56, v241\n"
"v_sub_f32 v57, v57, v241\n"
"v_sub_f32 v58, v58, v241\n"
"v_sub_f32 v59, v59, v241\n"
"v_sub_f32 v60, v60, v241\n"
"v_sub_f32 v61, v61, v241\n"
"v_sub_f32 v62, v62, v241\n"
"v_sub_f32 v63, v63, v241\n"
"v_sub_f32 v144, v144, v241\n"
"v_sub_f32 v145, v145, v241\n"
"v_sub_f32 v146, v146, v241\n"
"v_sub_f32 v147, v147, v241\n"
"v_sub_f32 v148, v148, v241\n"
"v_sub_f32 v149, v149, v241\n"
"v_sub_f32 v150, v150, v241\n"
"v_sub_f32 v151, v151, v241\n"
"v_sub_f32 v152, v152, v241\n"
"v_sub_f32 v153, v153, v241\n"
"v_sub_f32 v154, v154, v241\n"
"v_sub_f32 v155, v155, v241\n"
"v_sub_f32 v156, v156, v241\n"
"v_sub_f32 v157, v157, v241\n"
"v_sub_f32 v158, v158, v241\n"
"v_sub_f32 v159, v159, v241\n"
"v_mul_f32 v237, v244, v237\n"
"v_mul_f32 v238, v244, v238\n"
"s_mov_b32 s58, 1\n"
"s_nop 7\n"
"s_add_u32 s50, s50, 4\n"
"s_addc_u32 s51, s51, 0\n"
"s_setpc_b64 s[50:51]\n"
"Lslow1_%=:\n"
"s_nop 15\n"
"v_max_f32 v241, v64, v65\n"
"v_max3_f32 v241, v241, v66, v67\n"
"v_max3_f32 v241, v241, v68, v69\n"
"v_max3_f32 v241, v241, v70, v71\n"
"v_max3_f32 v241, v241, v72, v73\n"
"v_max3_f32 v241, v241, v74, v75\n"
"v_max3_f32 v241, v241, v76, v77\n"
"v_max3_f32 v241, v241, v78, v79\n"
"v_max3_f32 v241, v241, v96, v97\n"
"v_max3_f32 v241, v241, v98, v99\n"
"v_max3_f32 v241, v241, v100, v101\n"
"v_max3_f32 v241, v241, v102, v103\n"
"v_max3_f32 v241, v241, v104, v105\n"
"v_max3_f32 v241, v241, v106, v107\n"
"v_max3_f32 v241, v241, v108, v109\n"
"v_max3_f32 v241, v241, v110, v111\n"
"v_mov_b32 v242, v241\n"
"s_nop 1\n"
"v_permlane32_swap_b32 v241, v242\n"
"v_max3_f32 v241, v241, v242, 0\n"
"v_sub_f32 v242, 0, v241\n"
"v_exp_f32 v243, v242\n"
"v_sub_f32 v64, v64, v241\n"
"v_sub_f32 v65, v65, v241\n"
"v_sub_f32 v66, v66, v241\n"
"v_sub_f32 v67, v67, v241\n"
"v_sub_f32 v68, v68, v241\n"
"v_sub_f32 v69, v69, v241\n"
"v_sub_f32 v70, v70, v241\n"
"v_sub_f32 v71, v71, v241\n"
"v_sub_f32 v72, v72, v241\n"
"v_sub_f32 v73, v73, v241\n"
"v_sub_f32 v74, v74, v241\n"
"v_sub_f32 v75, v75, v241\n"
"v_sub_f32 v76, v76, v241\n"
"v_sub_f32 v77, v77, v241\n"
"v_sub_f32 v78, v78, v241\n"
"v_sub_f32 v79, v79, v241\n"
"v_sub_f32 v96, v96, v241\n"
"v_sub_f32 v97, v97, v241\n"
"v_sub_f32 v98, v98, v241\n"
"v_sub_f32 v99, v99, v241\n"
"v_sub_f32 v100, v100, v241\n"
"v_sub_f32 v101, v101, v241\n"
"v_sub_f32 v102, v102, v241\n"
"v_sub_f32 v103, v103, v241\n"
"v_sub_f32 v104, v104, v241\n"
"v_sub_f32 v105, v105, v241\n"
"v_sub_f32 v106, v106, v241\n"
"v_sub_f32 v107, v107, v241\n"
"v_sub_f32 v108, v108, v241\n"
"v_sub_f32 v109, v109, v241\n"
"v_sub_f32 v110, v110, v241\n"
"v_sub_f32 v111, v111, v241\n"
"v_sub_f32 v128, v128, v241\n"
"v_sub_f32 v129, v129, v241\n"
"v_sub_f32 v130, v130, v241\n"
"v_sub_f32 v131, v131, v241\n"
"v_sub_f32 v132, v132, v241\n"
"v_sub_f32 v133, v133, v241\n"
"v_sub_f32 v134, v134, v241\n"
"v_sub_f32 v135, v135, v241\n"
"v_sub_f32 v136, v136, v241\n"
"v_sub_f32 v137, v137, v241\n"
"v_sub_f32 v138, v138, v241\n"
"v_sub_f32 v139, v139, v241\n"
"v_sub_f32 v140, v140, v241\n"
"v_sub_f32 v141, v141, v241\n"
"v_sub_f32 v142, v142, v241\n"
"v_sub_f32 v143, v143, v241\n"
"v_mul_f32 v235, v243, v235\n"
"v_mul_f32 v236, v243, v236\n"
"v_max_f32 v241, v80, v81\n"
"v_max3_f32 v241, v241, v82, v83\n"
"v_max3_f32 v241, v241, v84, v85\n"
"v_max3_f32 v241, v241, v86, v87\n"
"v_max3_f32 v241, v241, v88, v89\n"
"v_max3_f32 v241, v241, v90, v91\n"
"v_max3_f32 v241, v241, v92, v93\n"
"v_max3_f32 v241, v241, v94, v95\n"
"v_max3_f32 v241, v241, v112, v113\n"
"v_max3_f32 v241, v241, v114, v115\n"
"v_max3_f32 v241, v241, v116, v117\n"
"v_max3_f32 v241, v241, v118, v119\n"
"v_max3_f32 v241, v241, v120, v121\n"
"v_max3_f32 v241, v241, v122, v123\n"
"v_max3_f32 v241, v241, v124, v125\n"
"v_max3_f32 v241, v241, v126, v127\n"
"v_mov_b32 v242, v241\n"
"s_nop 1\n"
"v_permlane32_swap_b32 v241, v242\n"
"v_max3_f32 v241, v241, v242, 0\n"
"v_sub_f32 v242, 0, v241\n"
"v_exp_f32 v244, v242\n"
"v_sub_f32 v80, v80, v241\n"
"v_sub_f32 v81, v81, v241\n"
"v_sub_f32 v82, v82, v241\n"
"v_sub_f32 v83, v83, v241\n"
"v_sub_f32 v84, v84, v241\n"
"v_sub_f32 v85, v85, v241\n"
"v_sub_f32 v86, v86, v241\n"
"v_sub_f32 v87, v87, v241\n"
"v_sub_f32 v88, v88, v241\n"
"v_sub_f32 v89, v89, v241\n"
"v_sub_f32 v90, v90, v241\n"
"v_sub_f32 v91, v91, v241\n"
"v_sub_f32 v92, v92, v241\n"
"v_sub_f32 v93, v93, v241\n"
"v_sub_f32 v94, v94, v241\n"
"v_sub_f32 v95, v95, v241\n"
"v_sub_f32 v112, v112, v241\n"
"v_sub_f32 v113, v113, v241\n"
"v_sub_f32 v114, v114, v241\n"
"v_sub_f32 v115, v115, v241\n"
"v_sub_f32 v116, v116, v241\n"
"v_sub_f32 v117, v117, v241\n"
"v_sub_f32 v118, v118, v241\n"
"v_sub_f32 v119, v119, v241\n"
"v_sub_f32 v120, v120, v241\n"
"v_sub_f32 v121, v121, v241\n"
"v_sub_f32 v122, v122, v241\n"
"v_sub_f32 v123, v123, v241\n"
"v_sub_f32 v124, v124, v241\n"
"v_sub_f32 v125, v125, v241\n"
"v_sub_f32 v126, v126, v241\n"
"v_sub_f32 v127, v127, v241\n"
"v_sub_f32 v144, v144, v241\n"
"v_sub_f32 v145, v145, v241\n"
"v_sub_f32 v146, v146, v241\n"
"v_sub_f32 v147, v147, v241\n"
"v_sub_f32 v148, v148, v241\n"
"v_sub_f32 v149, v149, v241\n"
"v_sub_f32 v150, v150, v241\n"
"v_sub_f32 v151, v151, v241\n"
"v_sub_f32 v152, v152, v241\n"
"v_sub_f32 v153, v153, v241\n"
"v_sub_f32 v154, v154, v241\n"
"v_sub_f32 v155, v155, v241\n"
"v_sub_f32 v156, v156, v241\n"
"v_sub_f32 v157, v157, v241\n"
"v_sub_f32 v158, v158, v241\n"
"v_sub_f32 v159, v159, v241\n"
"v_mul_f32 v237, v244, v237\n"
"v_mul_f32 v238, v244, v238\n"
"s_mov_b32 s58, 1\n"
"s_nop 7\n"
"s_add_u32 s50, s50, 4\n"
"s_addc_u32 s51, s51, 0\n"
"s_setpc_b64 s[50:51]\n"
"Lresc_%=:\n"
"s_nop 15\n"
"v_accvgpr_read_b32 v241, a0\n"
"v_mul_f32 v241, v243, v241\n"
"v_accvgpr_write_b32 a0, v241\n"
"v_accvgpr_read_b32 v242, a1\n"
"v_mul_f32 v242, v243, v242\n"
"v_accvgpr_write_b32 a1, v242\n"
"v_accvgpr_read_b32 v241, a2\n"
"v_mul_f32 v241, v243, v241\n"
"v_accvgpr_write_b32 a2, v241\n"
"v_accvgpr_read_b32 v242, a3\n"
"v_mul_f32 v242, v243, v242\n"
"v_accvgpr_write_b32 a3, v242\n"
"v_accvgpr_read_b32 v241, a4\n"
"v_mul_f32 v241, v243, v241\n"
"v_accvgpr_write_b32 a4, v241\n"
"v_accvgpr_read_b32 v242, a5\n"
"v_mul_f32 v242, v243, v242\n"
"v_accvgpr_write_b32 a5, v242\n"
"v_accvgpr_read_b32 v241, a6\n"
"v_mul_f32 v241, v243, v241\n"
"v_accvgpr_write_b32 a6, v241\n"
"v_accvgpr_read_b32 v242, a7\n"
"v_mul_f32 v242, v243, v242\n"
"v_accvgpr_write_b32 a7, v242\n"
"v_accvgpr_read_b32 v241, a8\n"
"v_mul_f32 v241, v243, v241\n"
"v_accvgpr_write_b32 a8, v241\n"
"v_accvgpr_read_b32 v242, a9\n"
"v_mul_f32 v242, v243, v242\n"
"v_accvgpr_write_b32 a9, v242\n"
"v_accvgpr_read_b32 v241, a10\n"
"v_mul_f32 v241, v243, v241\n"
"v_accvgpr_write_b32 a10, v241\n"
"v_accvgpr_read_b32 v242, a11\n"
"v_mul_f32 v242, v243, v242\n"
"v_accvgpr_write_b32 a11, v242\n"
"v_accvgpr_read_b32 v241, a12\n"
"v_mul_f32 v241, v243, v241\n"
"v_accvgpr_write_b32 a12, v241\n"
"v_accvgpr_read_b32 v242, a13\n"
"v_mul_f32 v242, v243, v242\n"
"v_accvgpr_write_b32 a13, v242\n"
"v_accvgpr_read_b32 v241, a14\n"
"v_mul_f32 v241, v243, v241\n"
"v_accvgpr_write_b32 a14, v241\n"
"v_accvgpr_read_b32 v242, a15\n"
"v_mul_f32 v242, v243, v242\n"
"v_accvgpr_write_b32 a15, v242\n"
"v_accvgpr_read_b32 v241, a16\n"
"v_mul_f32 v241, v244, v241\n"
"v_accvgpr_write_b32 a16, v241\n"
"v_accvgpr_read_b32 v242, a17\n"
"v_mul_f32 v242, v244, v242\n"
"v_accvgpr_write_b32 a17, v242\n"
"v_accvgpr_read_b32 v241, a18\n"
"v_mul_f32 v241, v244, v241\n"
"v_accvgpr_write_b32 a18, v241\n"
"v_accvgpr_read_b32 v242, a19\n"
"v_mul_f32 v242, v244, v242\n"
"v_accvgpr_write_b32 a19, v242\n"
"v_accvgpr_read_b32 v241, a20\n"
"v_mul_f32 v241, v244, v241\n"
"v_accvgpr_write_b32 a20, v241\n"
"v_accvgpr_read_b32 v242, a21\n"
"v_mul_f32 v242, v244, v242\n"
"v_accvgpr_write_b32 a21, v242\n"
"v_accvgpr_read_b32 v241, a22\n"
"v_mul_f32 v241, v244, v241\n"
"v_accvgpr_write_b32 a22, v241\n"
"v_accvgpr_read_b32 v242, a23\n"
"v_mul_f32 v242, v244, v242\n"
"v_accvgpr_write_b32 a23, v242\n"
"v_accvgpr_read_b32 v241, a24\n"
"v_mul_f32 v241, v244, v241\n"
"v_accvgpr_write_b32 a24, v241\n"
"v_accvgpr_read_b32 v242, a25\n"
"v_mul_f32 v242, v244, v242\n"
"v_accvgpr_write_b32 a25, v242\n"
"v_accvgpr_read_b32 v241, a26\n"
"v_mul_f32 v241, v244, v241\n"
"v_accvgpr_write_b32 a26, v241\n"
"v_accvgpr_read_b32 v242, a27\n"
"v_mul_f32 v242, v244, v242\n"
"v_accvgpr_write_b32 a27, v242\n"
"v_accvgpr_read_b32 v241, a28\n"
"v_mul_f32 v241, v244, v241\n"
"v_accvgpr_write_b32 a28, v241\n"
"v_accvgpr_read_b32 v242, a29\n"
"v_mul_f32 v242, v244, v242\n"
"v_accvgpr_write_b32 a29, v242\n"
"v_accvgpr_read_b32 v241, a30\n"
"v_mul_f32 v241, v244, v241\n"
"v_accvgpr_write_b32 a30, v241\n"
"v_accvgpr_read_b32 v242, a31\n"
"v_mul_f32 v242, v244, v242\n"
"v_accvgpr_write_b32 a31, v242\n"
"v_accvgpr_read_b32 v241, a32\n"
"v_mul_f32 v241, v243, v241\n"
"v_accvgpr_write_b32 a32, v241\n"
"v_accvgpr_read_b32 v242, a33\n"
"v_mul_f32 v242, v243, v242\n"
"v_accvgpr_write_b32 a33, v242\n"
"v_accvgpr_read_b32 v241, a34\n"
"v_mul_f32 v241, v243, v241\n"
"v_accvgpr_write_b32 a34, v241\n"
"v_accvgpr_read_b32 v242, a35\n"
"v_mul_f32 v242, v243, v242\n"
"v_accvgpr_write_b32 a35, v242\n"
"v_accvgpr_read_b32 v241, a36\n"
"v_mul_f32 v241, v243, v241\n"
"v_accvgpr_write_b32 a36, v241\n"
"v_accvgpr_read_b32 v242, a37\n"
"v_mul_f32 v242, v243, v242\n"
"v_accvgpr_write_b32 a37, v242\n"
"v_accvgpr_read_b32 v241, a38\n"
"v_mul_f32 v241, v243, v241\n"
"v_accvgpr_write_b32 a38, v241\n"
"v_accvgpr_read_b32 v242, a39\n"
"v_mul_f32 v242, v243, v242\n"
"v_accvgpr_write_b32 a39, v242\n"
"v_accvgpr_read_b32 v241, a40\n"
"v_mul_f32 v241, v243, v241\n"
"v_accvgpr_write_b32 a40, v241\n"
"v_accvgpr_read_b32 v242, a41\n"
"v_mul_f32 v242, v243, v242\n"
"v_accvgpr_write_b32 a41, v242\n"
"v_accvgpr_read_b32 v241, a42\n"
"v_mul_f32 v241, v243, v241\n"
"v_accvgpr_write_b32 a42, v241\n"
"v_accvgpr_read_b32 v242, a43\n"
"v_mul_f32 v242, v243, v242\n"
"v_accvgpr_write_b32 a43, v242\n"
"v_accvgpr_read_b32 v241, a44\n"
"v_mul_f32 v241, v243, v241\n"
"v_accvgpr_write_b32 a44, v241\n"
"v_accvgpr_read_b32 v242, a45\n"
"v_mul_f32 v242, v243, v242\n"
"v_accvgpr_write_b32 a45, v242\n"
"v_accvgpr_read_b32 v241, a46\n"
"v_mul_f32 v241, v243, v241\n"
"v_accvgpr_write_b32 a46, v241\n"
"v_accvgpr_read_b32 v242, a47\n"
"v_mul_f32 v242, v243, v242\n"
"v_accvgpr_write_b32 a47, v242\n"
"v_accvgpr_read_b32 v241, a48\n"
"v_mul_f32 v241, v244, v241\n"
"v_accvgpr_write_b32 a48, v241\n"
"v_accvgpr_read_b32 v242, a49\n"
"v_mul_f32 v242, v244, v242\n"
"v_accvgpr_write_b32 a49, v242\n"
"v_accvgpr_read_b32 v241, a50\n"
"v_mul_f32 v241, v244, v241\n"
"v_accvgpr_write_b32 a50, v241\n"
"v_accvgpr_read_b32 v242, a51\n"
"v_mul_f32 v242, v244, v242\n"
"v_accvgpr_write_b32 a51, v242\n"
"v_accvgpr_read_b32 v241, a52\n"
"v_mul_f32 v241, v244, v241\n"
"v_accvgpr_write_b32 a52, v241\n"
"v_accvgpr_read_b32 v242, a53\n"
"v_mul_f32 v242, v244, v242\n"
"v_accvgpr_write_b32 a53, v242\n"
"v_accvgpr_read_b32 v241, a54\n"
"v_mul_f32 v241, v244, v241\n"
"v_accvgpr_write_b32 a54, v241\n"
"v_accvgpr_read_b32 v242, a55\n"
"v_mul_f32 v242, v244, v242\n"
"v_accvgpr_write_b32 a55, v242\n"
"v_accvgpr_read_b32 v241, a56\n"
"v_mul_f32 v241, v244, v241\n"
"v_accvgpr_write_b32 a56, v241\n"
"v_accvgpr_read_b32 v242, a57\n"
"v_mul_f32 v242, v244, v242\n"
"v_accvgpr_write_b32 a57, v242\n"
"v_accvgpr_read_b32 v241, a58\n"
"v_mul_f32 v241, v244, v241\n"
"v_accvgpr_write_b32 a58, v241\n"
"v_accvgpr_read_b32 v242, a59\n"
"v_mul_f32 v242, v244, v242\n"
"v_accvgpr_write_b32 a59, v242\n"
"v_accvgpr_read_b32 v241, a60\n"
"v_mul_f32 v241, v244, v241\n"
"v_accvgpr_write_b32 a60, v241\n"
"v_accvgpr_read_b32 v242, a61\n"
"v_mul_f32 v242, v244, v242\n"
"v_accvgpr_write_b32 a61, v242\n"
"v_accvgpr_read_b32 v241, a62\n"
"v_mul_f32 v241, v244, v241\n"
"v_accvgpr_write_b32 a62, v241\n"
"v_accvgpr_read_b32 v242, a63\n"
"v_mul_f32 v242, v244, v242\n"
"v_accvgpr_write_b32 a63, v242\n"
"v_accvgpr_read_b32 v241, a64\n"
"v_mul_f32 v241, v243, v241\n"
"v_accvgpr_write_b32 a64, v241\n"
"v_accvgpr_read_b32 v242, a65\n"
"v_mul_f32 v242, v243, v242\n"
"v_accvgpr_write_b32 a65, v242\n"
"v_accvgpr_read_b32 v241, a66\n"
"v_mul_f32 v241, v243, v241\n"
"v_accvgpr_write_b32 a66, v241\n"
"v_accvgpr_read_b32 v242, a67\n"
"v_mul_f32 v242, v243, v242\n"
"v_accvgpr_write_b32 a67, v242\n"
"v_accvgpr_read_b32 v241, a68\n"
"v_mul_f32 v241, v243, v241\n"
"v_accvgpr_write_b32 a68, v241\n"
"v_accvgpr_read_b32 v242, a69\n"
"v_mul_f32 v242, v243, v242\n"
"v_accvgpr_write_b32 a69, v242\n"
"v_accvgpr_read_b32 v241, a70\n"
"v_mul_f32 v241, v243, v241\n"
"v_accvgpr_write_b32 a70, v241\n"
"v_accvgpr_read_b32 v242, a71\n"
"v_mul_f32 v242, v243, v242\n"
"v_accvgpr_write_b32 a71, v242\n"
"v_accvgpr_read_b32 v241, a72\n"
"v_mul_f32 v241, v243, v241\n"
"v_accvgpr_write_b32 a72, v241\n"
"v_accvgpr_read_b32 v242, a73\n"
"v_mul_f32 v242, v243, v242\n"
"v_accvgpr_write_b32 a73, v242\n"
"v_accvgpr_read_b32 v241, a74\n"
"v_mul_f32 v241, v243, v241\n"
"v_accvgpr_write_b32 a74, v241\n"
"v_accvgpr_read_b32 v242, a75\n"
"v_mul_f32 v242, v243, v242\n"
"v_accvgpr_write_b32 a75, v242\n"
"v_accvgpr_read_b32 v241, a76\n"
"v_mul_f32 v241, v243, v241\n"
"v_accvgpr_write_b32 a76, v241\n"
"v_accvgpr_read_b32 v242, a77\n"
"v_mul_f32 v242, v243, v242\n"
"v_accvgpr_write_b32 a77, v242\n"
"v_accvgpr_read_b32 v241, a78\n"
"v_mul_f32 v241, v243, v241\n"
"v_accvgpr_write_b32 a78, v241\n"
"v_accvgpr_read_b32 v242, a79\n"
"v_mul_f32 v242, v243, v242\n"
"v_accvgpr_write_b32 a79, v242\n"
"v_accvgpr_read_b32 v241, a80\n"
"v_mul_f32 v241, v244, v241\n"
"v_accvgpr_write_b32 a80, v241\n"
"v_accvgpr_read_b32 v242, a81\n"
"v_mul_f32 v242, v244, v242\n"
"v_accvgpr_write_b32 a81, v242\n"
"v_accvgpr_read_b32 v241, a82\n"
"v_mul_f32 v241, v244, v241\n"
"v_accvgpr_write_b32 a82, v241\n"
"v_accvgpr_read_b32 v242, a83\n"
"v_mul_f32 v242, v244, v242\n"
"v_accvgpr_write_b32 a83, v242\n"
"v_accvgpr_read_b32 v241, a84\n"
"v_mul_f32 v241, v244, v241\n"
"v_accvgpr_write_b32 a84, v241\n"
"v_accvgpr_read_b32 v242, a85\n"
"v_mul_f32 v242, v244, v242\n"
"v_accvgpr_write_b32 a85, v242\n"
"v_accvgpr_read_b32 v241, a86\n"
"v_mul_f32 v241, v244, v241\n"
"v_accvgpr_write_b32 a86, v241\n"
"v_accvgpr_read_b32 v242, a87\n"
"v_mul_f32 v242, v244, v242\n"
"v_accvgpr_write_b32 a87, v242\n"
"v_accvgpr_read_b32 v241, a88\n"
"v_mul_f32 v241, v244, v241\n"
"v_accvgpr_write_b32 a88, v241\n"
"v_accvgpr_read_b32 v242, a89\n"
"v_mul_f32 v242, v244, v242\n"
"v_accvgpr_write_b32 a89, v242\n"
"v_accvgpr_read_b32 v241, a90\n"
"v_mul_f32 v241, v244, v241\n"
"v_accvgpr_write_b32 a90, v241\n"
"v_accvgpr_read_b32 v242, a91\n"
"v_mul_f32 v242, v244, v242\n"
"v_accvgpr_write_b32 a91, v242\n"
"v_accvgpr_read_b32 v241, a92\n"
"v_mul_f32 v241, v244, v241\n"
"v_accvgpr_write_b32 a92, v241\n"
"v_accvgpr_read_b32 v242, a93\n"
"v_mul_f32 v242, v244, v242\n"
"v_accvgpr_write_b32 a93, v242\n"
"v_accvgpr_read_b32 v241, a94\n"
"v_mul_f32 v241, v244, v241\n"
"v_accvgpr_write_b32 a94, v241\n"
"v_accvgpr_read_b32 v242, a95\n"
"v_mul_f32 v242, v244, v242\n"
"v_accvgpr_write_b32 a95, v242\n"
"v_accvgpr_read_b32 v241, a96\n"
"v_mul_f32 v241, v243, v241\n"
"v_accvgpr_write_b32 a96, v241\n"
"v_accvgpr_read_b32 v242, a97\n"
"v_mul_f32 v242, v243, v242\n"
"v_accvgpr_write_b32 a97, v242\n"
"v_accvgpr_read_b32 v241, a98\n"
"v_mul_f32 v241, v243, v241\n"
"v_accvgpr_write_b32 a98, v241\n"
"v_accvgpr_read_b32 v242, a99\n"
"v_mul_f32 v242, v243, v242\n"
"v_accvgpr_write_b32 a99, v242\n"
"v_accvgpr_read_b32 v241, a100\n"
"v_mul_f32 v241, v243, v241\n"
"v_accvgpr_write_b32 a100, v241\n"
"v_accvgpr_read_b32 v242, a101\n"
"v_mul_f32 v242, v243, v242\n"
"v_accvgpr_write_b32 a101, v242\n"
"v_accvgpr_read_b32 v241, a102\n"
"v_mul_f32 v241, v243, v241\n"
"v_accvgpr_write_b32 a102, v241\n"
"v_accvgpr_read_b32 v242, a103\n"
"v_mul_f32 v242, v243, v242\n"
"v_accvgpr_write_b32 a103, v242\n"
"v_accvgpr_read_b32 v241, a104\n"
"v_mul_f32 v241, v243, v241\n"
"v_accvgpr_write_b32 a104, v241\n"
"v_accvgpr_read_b32 v242, a105\n"
"v_mul_f32 v242, v243, v242\n"
"v_accvgpr_write_b32 a105, v242\n"
"v_accvgpr_read_b32 v241, a106\n"
"v_mul_f32 v241, v243, v241\n"
"v_accvgpr_write_b32 a106, v241\n"
"v_accvgpr_read_b32 v242, a107\n"
"v_mul_f32 v242, v243, v242\n"
"v_accvgpr_write_b32 a107, v242\n"
"v_accvgpr_read_b32 v241, a108\n"
"v_mul_f32 v241, v243, v241\n"
"v_accvgpr_write_b32 a108, v241\n"
"v_accvgpr_read_b32 v242, a109\n"
"v_mul_f32 v242, v243, v242\n"
"v_accvgpr_write_b32 a109, v242\n"
"v_accvgpr_read_b32 v241, a110\n"
"v_mul_f32 v241, v243, v241\n"
"v_accvgpr_write_b32 a110, v241\n"
"v_accvgpr_read_b32 v242, a111\n"
"v_mul_f32 v242, v243, v242\n"
"v_accvgpr_write_b32 a111, v242\n"
"v_accvgpr_read_b32 v241, a112\n"
"v_mul_f32 v241, v244, v241\n"
"v_accvgpr_write_b32 a112, v241\n"
"v_accvgpr_read_b32 v242, a113\n"
"v_mul_f32 v242, v244, v242\n"
"v_accvgpr_write_b32 a113, v242\n"
"v_accvgpr_read_b32 v241, a114\n"
"v_mul_f32 v241, v244, v241\n"
"v_accvgpr_write_b32 a114, v241\n"
"v_accvgpr_read_b32 v242, a115\n"
"v_mul_f32 v242, v244, v242\n"
"v_accvgpr_write_b32 a115, v242\n"
"v_accvgpr_read_b32 v241, a116\n"
"v_mul_f32 v241, v244, v241\n"
"v_accvgpr_write_b32 a116, v241\n"
"v_accvgpr_read_b32 v242, a117\n"
"v_mul_f32 v242, v244, v242\n"
"v_accvgpr_write_b32 a117, v242\n"
"v_accvgpr_read_b32 v241, a118\n"
"v_mul_f32 v241, v244, v241\n"
"v_accvgpr_write_b32 a118, v241\n"
"v_accvgpr_read_b32 v242, a119\n"
"v_mul_f32 v242, v244, v242\n"
"v_accvgpr_write_b32 a119, v242\n"
"v_accvgpr_read_b32 v241, a120\n"
"v_mul_f32 v241, v244, v241\n"
"v_accvgpr_write_b32 a120, v241\n"
"v_accvgpr_read_b32 v242, a121\n"
"v_mul_f32 v242, v244, v242\n"
"v_accvgpr_write_b32 a121, v242\n"
"v_accvgpr_read_b32 v241, a122\n"
"v_mul_f32 v241, v244, v241\n"
"v_accvgpr_write_b32 a122, v241\n"
"v_accvgpr_read_b32 v242, a123\n"
"v_mul_f32 v242, v244, v242\n"
"v_accvgpr_write_b32 a123, v242\n"
"v_accvgpr_read_b32 v241, a124\n"
"v_mul_f32 v241, v244, v241\n"
"v_accvgpr_write_b32 a124, v241\n"
"v_accvgpr_read_b32 v242, a125\n"
"v_mul_f32 v242, v244, v242\n"
"v_accvgpr_write_b32 a125, v242\n"
"v_accvgpr_read_b32 v241, a126\n"
"v_mul_f32 v241, v244, v241\n"
"v_accvgpr_write_b32 a126, v241\n"
"v_accvgpr_read_b32 v242, a127\n"
"v_mul_f32 v242, v244, v242\n"
"v_accvgpr_write_b32 a127, v242\n"
"s_mov_b32 s58, 0\n"
"s_nop 7\n"
"s_add_u32 s50, s50, 4\n"
"s_addc_u32 s51, s51, 0\n"
"s_setpc_b64 s[50:51]\n"
  :: "s"(__builtin_amdgcn_kernarg_segment_ptr()), "s"(blockIdx.x), "v"(tid), "s"((unsigned)(size_t)(__attribute__((address_space(3))) char*)lds)
  : "v1","v2","v3","v4","v5","v6","v7","v8","v9","v10","v11","v12","v13","v14","v15","v16","v17","v18","v19","v20","v21","v22","v23","v24","v25","v26","v27","v28","v29","v30","v31","v32","v33","v34","v35","v36","v37","v38","v39","v40","v41","v42","v43","v44","v45","v46","v47","v48","v49","v50","v51","v52","v53","v54","v55","v56","v57","v58","v59","v60","v61","v62","v63","v64","v65","v66","v67","v68","v69","v70","v71","v72","v73","v74","v75","v76","v77","v78","v79","v80","v81","v82","v83","v84","v85","v86","v87","v88","v89","v90","v91","v92","v93","v94","v95","v96","v97","v98","v99","v100","v101","v102","v103","v104","v105","v106","v107","v108","v109","v110","v111","v112","v113","v114","v115","v116","v117","v118","v119","v120","v121","v122","v123","v124","v125","v126","v127","v128","v129","v130","v131","v132","v133","v134","v135","v136","v137","v138","v139","v140","v141","v142","v143","v144","v145","v146","v147","v148","v149","v150","v151","v152","v153","v154","v155","v156","v157","v158","v159","v160","v161","v162","v163","v164","v165","v166","v167","v168","v169","v170","v171","v172","v173","v174","v175","v176","v177","v178","v179","v180","v181","v182","v183","v184","v185","v186","v187","v188","v189","v190","v191","v192","v193","v194","v195","v196","v197","v198","v199","v200","v201","v202","v203","v204","v205","v206","v207","v208","v209","v210","v211","v212","v213","v214","v215","v216","v217","v218","v219","v220","v221","v222","v223","v224","v225","v226","v227","v228","v229","v230","v231","v232","v233","v234","v235","v236","v237","v238","v239","v240","v241","v242","v243","v244","v245","v246","v247","v248","v249","v250","v251","v252","v253","v254","v255","a0","a1","a2","a3","a4","a5","a6","a7","a8","a9","a10","a11","a12","a13","a14","a15","a16","a17","a18","a19","a20","a21","a22","a23","a24","a25","a26","a27","a28","a29","a30","a31","a32","a33","a34","a35","a36","a37","a38","a39","a40","a41","a42","a43","a44","a45","a46","a47","a48","a49","a50","a51","a52","a53","a54","a55","a56","a57","a58","a59","a60","a61","a62","a63","a64","a65","a66","a67","a68","a69","a70","a71","a72","a73","a74","a75","a76","a77","a78","a79","a80","a81","a82","a83","a84","a85","a86","a87","a88","a89","a90","a91","a92","a93","a94","a95","a96","a97","a98","a99","a100","a101","a102","a103","a104","a105","a106","a107","a108","a109","a110","a111","a112","a113","a114","a115","a116","a117","a118","a119","a120","a121","a122","a123","a124","a125","a126","a127","a128","a129","a130","a131","a132","a133","a134","a135","a136","a137","a138","a139","a140","a141","a142","a143","a144","a145","a146","a147","a148","a149","a150","a151","a152","a153","a154","a155","a156","a157","a158","a159","a160","a161","a162","a163","a164","a165","a166","a167","a168","a169","a170","a171","a172","a173","a174","a175","a176","a177","a178","a179","a180","a181","a182","a183","a184","a185","a186","a187","a188","a189","a190","a191","a192","a193","a194","a195","a196","a197","a198","a199","a200","a201","a202","a203","a204","a205","a206","a207","a208","a209","a210","a211","a212","a213","a214","a215","a216","a217","a218","a219","a220","a221","a222","a223","a224","a225","a226","a227","a228","a229","a230","a231","a232","a233","a234","a235","a236","a237","a238","a239","a240","a241","a242","a243","a244","a245","a246","a247","a248","a249","a250","a251","a252","a253","a254","a255","s4","s5","s6","s7","s8","s9","s10","s11","s12","s13","s14","s15","s16","s17","s18","s19","s20","s21","s22","s23","s24","s25","s26","s27","s28","s29","s30","s31","s32","s33","s34","s35","s36","s37","s38","s39","s40","s41","s42","s43","s44","s45","s46","s47","s48","s49","s50","s51","s52","s53","s54","s55","s56","s57","s58","s59","s60","s61","s62","s63","s64","s65","s66","s67","s68","s69","s70","s71","s72","s73","s74","s75","s76","s77","s78","s79","s80","s81","s82","s83","s84","s85","s86","s87","s88","s89","s90","s91","s92","s93","s94","s95","s96","s97","s98","s99","s100","s101","vcc","scc","memory");
}

extern "C" void kernel_launch(void* const* d_in, const int* in_sizes, int n_in, void* d_out, int out_size, void* d_ws, size_t ws_size, hipStream_t stream) {
  gqa_fused_kernel<<<256, 256, 0, stream>>>(d_in[0], d_in[1], d_in[2], d_in[3], d_in[4], d_out, d_ws);
}
```
